```python
import torch

def _softmax(x, axis=-1):
    return torch.softmax(x, dim=axis)
def _gelu(x):
    return torch.nn.functional.gelu(x, approximate="tanh")
def _silu(x):
    return torch.nn.functional.silu(x)
def _sigmoid(x):
    return torch.sigmoid(x)
def _layernorm(x, axis=-1, eps=1e-5):
    m = x.mean(dim=axis, keepdim=True)
    v = x.var(dim=axis, keepdim=True, unbiased=False)
    return (x - m) / torch.sqrt(v + eps)
def _scatter_add(tgt, idx, src, axis=0):
    return tgt.clone().index_add_(axis, idx, src)
def _segment_sum(src, seg, n):
    out = torch.zeros((n,) + src.shape[1:], dtype=src.dtype)
    return out.index_add_(0, seg, src)
def _pad(x, pw):
    flat = tuple(v for pair in reversed(pw) for v in pair)
    return torch.nn.functional.pad(x, flat)

kernel_name = 'parallel_sum__sliding_local_dilated__dense_activation_relu__prompt'

def setup_inputs():

    _w = (lambda a: (lambda t: t.to(torch.bfloat16) if t.is_floating_point() else t)(torch.as_tensor(a)))
    def _A_setup():
        B, S, D = 1, 2048, 1024
        return {
            "x": _w((lambda *s: torch.randn(*s, dtype=torch.bfloat16) * 0.05)(B, S, D)),
            "wqkv": _w((lambda *s: torch.randn(*s, dtype=torch.bfloat16) * 0.05)(D, 3 * D)),
            "wo": _w((lambda *s: torch.randn(*s, dtype=torch.bfloat16) * 0.05)(D, D)),
        }
    _A_in = _A_setup()

    def _B_setup():
        B, S, D, Dff = 1, 2048, 1024, 3584
        return {
            "x": _w((lambda *s: torch.randn(*s, dtype=torch.bfloat16) * 0.05)(B, S, D)),
            "w1": _w((lambda *s: torch.randn(*s, dtype=torch.bfloat16) * 0.05)(D, Dff)),
            "w2": _w((lambda *s: torch.randn(*s, dtype=torch.bfloat16) * 0.05)(Dff, D)),
        }
    _B_in = _B_setup()

    return {
        "A_x": _A_in["x"],
        "A_wqkv": _A_in["wqkv"],
        "A_wo": _A_in["wo"],
        "B_x": _B_in["x"],
        "B_w1": _B_in["w1"],
        "B_w2": _B_in["w2"],
    }

def reference(A_x, A_wqkv, A_wo, B_x, B_w1, B_w2):
    def _A_ref(x, wqkv, wo):
        B, S, D = x.shape
        H = 32; Dh = D // H
        W, dil = 8, 2
        qkv = (lambda x, *d: x.permute(*d))((x @ wqkv).reshape(B, S, 3, H, Dh), 2, 0, 3, 1, 4)
        q, k, v = qkv[0], qkv[1], qkv[2]
        scale = Dh ** -0.5
        pos = torch.arange(S)
        off = torch.arange(W) * dil
        idx = pos[:, None] - off[None, :]
        mask = idx >= 0
        idx_c = torch.where(mask, idx, 0).reshape(-1)
        kg = k[:, :, idx_c, :].reshape(B, H, S, W, Dh)
        vg = v[:, :, idx_c, :].reshape(B, H, S, W, Dh)
        logits = torch.einsum("bhsd,bhswd->bhsw", q, kg) * scale
        logits = torch.where(mask[None, None, :, :], logits, -1e9)
        attn = _softmax(logits, axis=-1)
        o = torch.einsum("bhsw,bhswd->bhsd", attn, vg)
        return (lambda x, *d: x.permute(*d))(o, 0, 2, 1, 3).reshape(B, S, D) @ wo

    def _B_ref(x, w1, w2):
        h = torch.einsum("bsd,df->bsf", x, w1)
        zero = torch.zeros_like(h)
        h = torch.maximum(h, zero)
        return torch.einsum("bsf,fd->bsd", h, w2)

    x = A_x
    xn = _layernorm(x, axis=-1)
    return x + _A_ref(xn, A_wqkv, A_wo) + _B_ref(xn, B_w1, B_w2)
```

```cpp
#include <hip/hip_runtime.h>
#include <hip/hip_bf16.h>
#include <stdint.h>

#define LAS __attribute__((address_space(3)))
#define GAS __attribute__((address_space(1)))
typedef __attribute__((ext_vector_type(8))) short bf16x8;
typedef __attribute__((ext_vector_type(4))) short s16x4;
typedef __attribute__((ext_vector_type(4))) float f32x4;
typedef __attribute__((ext_vector_type(2))) float f32x2;
typedef __attribute__((ext_vector_type(2))) __bf16 bf16x2_t;
typedef __attribute__((ext_vector_type(4))) unsigned u32x4;
typedef __attribute__((ext_vector_type(2))) unsigned u32x2;

constexpr int S_ = 2048, D_ = 1024, DFF = 3584, KACT = D_ + DFF;

#define WAIT_V(n) asm volatile("s_waitcnt vmcnt(" #n ")" ::: "memory")
#define WAIT_L(n) asm volatile("s_waitcnt lgkmcnt(" #n ")" ::: "memory")
#define BAR __builtin_amdgcn_s_barrier()
#define SCHED __builtin_amdgcn_sched_barrier(0)
#define GLDS(src, dst) __builtin_amdgcn_global_load_lds((const unsigned*)(src), (LAS unsigned*)(dst), 16, 0, 0)

__device__ __forceinline__ void glds_s(const void* sbase, unsigned voff, unsigned ldsaddr) {
    asm volatile("s_mov_b32 m0, %2\n\ts_nop 0\n\tglobal_load_lds_dwordx4 %0, %1" ::"v"(voff), "s"(sbase), "s"(ldsaddr) : "memory");
}
__device__ __forceinline__ unsigned pack_bf16(float a, float b) {
    f32x2 v = {a, b};
    bf16x2_t r = __builtin_convertvector(v, bf16x2_t);
    return __builtin_bit_cast(unsigned, r);
}
__device__ __forceinline__ float bf_lo(unsigned u) { return __builtin_bit_cast(float, u << 16); }
__device__ __forceinline__ float bf_hi(unsigned u) { return __builtin_bit_cast(float, u & 0xffff0000u); }
__device__ __forceinline__ float rbf(float a) { return bf_lo(pack_bf16(a, a)); }
__device__ __forceinline__ bf16x8 lds_b128(LAS unsigned char* p) { return *(LAS bf16x8*)p; }
__device__ __forceinline__ bf16x8 lds_tr(LAS unsigned char* p, int hi_off) {
    s16x4 lo = __builtin_amdgcn_ds_read_tr16_b64_v4i16((LAS s16x4*)p);
    s16x4 hi = __builtin_amdgcn_ds_read_tr16_b64_v4i16((LAS s16x4*)(p + hi_off));
    return __builtin_shufflevector(lo, hi, 0, 1, 2, 3, 4, 5, 6, 7);
}
__device__ __forceinline__ f32x4 mfma16(bf16x8 a, bf16x8 b, f32x4 c) {
    return __builtin_amdgcn_mfma_f32_16x16x32_bf16(a, b, c, 0, 0, 0);
}
__device__ __forceinline__ f32x4 sel4(bool c, f32x4 a, f32x4 b) {
    f32x4 r;
#pragma unroll
    for (int e = 0; e < 4; ++e) r[e] = c ? a[e] : b[e];
    return r;
}
__device__ __forceinline__ float dot2bf(unsigned a, unsigned b, float c) {
    return __builtin_amdgcn_fdot2_f32_bf16(__builtin_bit_cast(bf16x2_t, a), __builtin_bit_cast(bf16x2_t, b), c, false);
}

__global__ __launch_bounds__(256) void ln_kernel(const __hip_bfloat16* __restrict__ x, __hip_bfloat16* __restrict__ xn,
                                                 unsigned* __restrict__ flags) {
    const int lane = threadIdx.x & 63, row = blockIdx.x * 4 + (threadIdx.x >> 6);
    if (blockIdx.x == 0) flags[threadIdx.x] = 0u;
    const u32x4* src = (const u32x4*)(x + (size_t)row * D_);
    u32x4 v0 = src[lane], v1 = src[64 + lane];
    float f[16];
#pragma unroll
    for (int i = 0; i < 4; ++i) { f[2 * i] = bf_lo(v0[i]); f[2 * i + 1] = bf_hi(v0[i]); f[8 + 2 * i] = bf_lo(v1[i]); f[9 + 2 * i] = bf_hi(v1[i]); }
    float s = 0.f;
#pragma unroll
    for (int i = 0; i < 16; ++i) s += f[i];
#pragma unroll
    for (int o = 32; o >= 1; o >>= 1) s += __shfl_xor(s, o);
    const float mean = s * (1.0f / D_);
    float q = 0.f;
#pragma unroll
    for (int i = 0; i < 16; ++i) { const float d = f[i] - mean; q += d * d; }
#pragma unroll
    for (int o = 32; o >= 1; o >>= 1) q += __shfl_xor(q, o);
    const float mb = rbf(mean);
    const float sd = rbf(sqrtf(rbf(rbf(q * (1.0f / D_)) + 1e-5f)));
    u32x4 o0, o1;
#pragma unroll
    for (int i = 0; i < 4; ++i) {
        o0[i] = pack_bf16(rbf(f[2 * i] - mb) / sd, rbf(f[2 * i + 1] - mb) / sd);
        o1[i] = pack_bf16(rbf(f[8 + 2 * i] - mb) / sd, rbf(f[9 + 2 * i] - mb) / sd);
    }
    u32x4* dst = (u32x4*)(xn + (size_t)row * D_);
    dst[lane] = o0; dst[64 + lane] = o1;
}

constexpr int G1_LDS = 128000;
constexpr int ATT_HEAD = 64000, ATT_K = 20480, ATT_V = 20480 + 21760, ATT_RS = 80;

template <bool QKV>
__device__ __forceinline__ void gemm1_body(LAS unsigned char* lds, const __hip_bfloat16* __restrict__ xn,
                                           const __hip_bfloat16* __restrict__ w, __hip_bfloat16* __restrict__ act,
                                           const int mt, const int ct) {
    constexpr int LDB = QKV ? 3 * D_ : DFF;
    const int tid = threadIdx.x, wid = __builtin_amdgcn_readfirstlane(tid >> 6), lane = tid & 63;
    const int G = wid >> 2, wq = wid & 3, fr = lane & 15, fq = lane >> 4;
    const int m0 = mt * 256;
    int cb0, cstep, cex;
    if (QKV) { cb0 = ct * 64; cstep = D_; cex = 0; }
    else { const int n0 = ct * 224; cb0 = n0 + ((ct & 1) ? 32 : 0); cstep = 64; cex = (ct & 1) ? n0 : n0 + 192; }

    const unsigned voffA = (unsigned)((8 * wid + (lane >> 3)) * (D_ * 2) + (((lane & 7) ^ ((4 * wid + (lane >> 4)) & 7)) << 4));
    const unsigned voffB = (unsigned)((8 * wid + (lane >> 3)) * (LDB * 2) + (((lane & 7) ^ ((((lane >> 4) & 1) + 2 * (wid & 1)) << 1)) << 4));
    unsigned voffE;
    if (QKV) voffE = (unsigned)((2 * wid + ((lane >> 3) & 1)) * (D_ * 2) + (((lane & 7) ^ wid) << 4));
    else voffE = (unsigned)((8 * wid + ((lane >> 2) & 7)) * (LDB * 2) + ((((lane >> 1) & 1) ^ (wid & 1)) << 5) + ((lane & 1) << 4));
    const char* gA = (const char*)xn + (size_t)m0 * (D_ * 2);
    const char* gB = (const char*)w + (size_t)cb0 * 2;
    const char* gE = QKV ? (const char*)xn + (size_t)(m0 >= 16 ? m0 - 16 : 0) * (D_ * 2) : (const char*)w + (size_t)cex * 2;
    constexpr size_t KSA = 128, KSB = (size_t)64 * LDB * 2, KSE = QKV ? KSA : KSB;
    LAS unsigned char* ldsw = lds + wid * 1024;
#define STAGE_X(b, t) do { \
        _Pragma("unroll") for (int _j = 0; _j < 3; ++_j) GLDS(gB + (size_t)(t) * KSB + (size_t)_j * cstep * 2 + voffB, ldsw + 65536 + (b) * 24576 + _j * 8192); \
        if (lane < (QKV ? 16 : 32)) GLDS(gE + (size_t)(t) * KSE + voffE, lds + 114688 + (b) * 4096 + wid * (QKV ? 256 : 512)); \
        _Pragma("unroll") for (int _u = 0; _u < 2; ++_u) GLDS(gA + (size_t)(t) * KSA + (size_t)_u * (64 * D_ * 2) + voffA, ldsw + (b) * 16384 + _u * 8192); \
    } while (0)
#define STAGE_Y(b, t) do { \
        _Pragma("unroll") for (int _u = 0; _u < 2; ++_u) GLDS(gA + (size_t)(t) * KSA + (size_t)(2 + _u) * (64 * D_ * 2) + voffA, ldsw + 32768 + (b) * 16384 + _u * 8192); \
    } while (0)

    const int q4 = (lane & 15) >> 2, p4 = lane & 3;
    const int aswz = (fq ^ ((fr >> 1) & 7)) << 4;
    LAS unsigned char* pA[2] = {lds + (wq * 32 + fr) * 128 + aswz, lds + (wq * 32 + fr) * 128 + (aswz ^ 64)};
    const int bsw = ((q4 >> 1) & 1) + 2 * (fq & 1);
    const int bbase = 65536 + fq * 1024 + q4 * 128 + p4 * 8;
    LAS unsigned char* pB[2] = {lds + bbase + (((2 * G) ^ bsw) << 5), lds + bbase + (((2 * G + 1) ^ bsw) << 5)};
    LAS unsigned char* pEB;
    LAS unsigned char* pEA[2];
    if (QKV) {
        pEB = lds + bbase + (1 + (wq >> 1)) * 8192 + (((2 * G + (wq & 1)) ^ bsw) << 5);
        pEA[0] = lds + 114688 + fr * 128 + aswz; pEA[1] = lds + 114688 + fr * 128 + (aswz ^ 64);
    } else {
        pEB = lds + 114688 + fq * 512 + q4 * 64 + ((G ^ (fq & 1)) << 5) + p4 * 8;
        pEA[0] = pEA[1] = lds;
    }

    f32x4 acc[2][2][6], accE[QKV ? 1 : 4];
#pragma unroll
    for (int h = 0; h < 2; ++h)
#pragma unroll
        for (int i = 0; i < 2; ++i)
#pragma unroll
            for (int c = 0; c < 6; ++c) acc[h][i][c] = (f32x4){0.f, 0.f, 0.f, 0.f};
#pragma unroll
    for (int i = 0; i < (QKV ? 1 : 4); ++i) accE[i] = (f32x4){0.f, 0.f, 0.f, 0.f};
    bf16x8 Af[2][2], Bf[6][2], Be[2], Ae[2];

#define LOAD_B(b) do { \
        _Pragma("unroll") for (int _k = 0; _k < 2; ++_k) { \
            _Pragma("unroll") for (int _c = 0; _c < 6; ++_c) Bf[_c][_k] = lds_tr(pB[_c & 1] + (b) * 24576 + (_c >> 1) * 8192 + _k * 4096, 512); \
            if (QKV) { Be[_k] = lds_tr(pEB + (b) * 24576 + _k * 4096, 512); Ae[_k] = lds_b128(pEA[_k] + (b) * 4096); } \
            else Be[_k] = lds_tr(pEB + (b) * 4096 + _k * 2048, 256); \
        } } while (0)
#define LOAD_A(b, h) do { \
        _Pragma("unroll") for (int _k = 0; _k < 2; ++_k) _Pragma("unroll") for (int _i = 0; _i < 2; ++_i) \
            Af[_i][_k] = lds_b128(pA[_k] + (h) * 32768 + (b) * 16384 + _i * 2048); \
    } while (0)
#define MMA(h) do { __builtin_amdgcn_s_setprio(1); \
        _Pragma("unroll") for (int _k = 0; _k < 2; ++_k) { \
            _Pragma("unroll") for (int _c = 0; _c < 6; ++_c) _Pragma("unroll") for (int _i = 0; _i < 2; ++_i) \
                acc[h][_i][_c] = mfma16(Bf[_c][_k], Af[_i][_k], acc[h][_i][_c]); \
            if (QKV) { if ((h) == 0) accE[0] = mfma16(Be[_k], Ae[_k], accE[0]); } \
            else { _Pragma("unroll") for (int _i = 0; _i < 2; ++_i) accE[(QKV ? 0 : (h) * 2 + _i)] = mfma16(Be[_k], Af[_i][_k], accE[(QKV ? 0 : (h) * 2 + _i)]); } \
        } __builtin_amdgcn_s_setprio(0); } while (0)

    constexpr int NT = D_ / 64;
    STAGE_X(0, 0); STAGE_Y(0, 0);
    if (G == 1) BAR;
    WAIT_V(2); BAR;
    STAGE_X(1, 1);
    WAIT_V(6); BAR;
    for (int t = 0; t < NT - 2; t += 2) {
        LOAD_B(0); LOAD_A(0, 0); STAGE_Y(1, t + 1);
        WAIT_V(8); WAIT_L(0); BAR; MMA(0); BAR; SCHED;
        LOAD_A(0, 1); STAGE_X(0, t + 2);
        WAIT_V(8); WAIT_L(0); BAR; MMA(1); BAR; SCHED;
        LOAD_B(1); LOAD_A(1, 0); STAGE_Y(0, t + 2);
        WAIT_V(8); WAIT_L(0); BAR; MMA(0); BAR; SCHED;
        LOAD_A(1, 1); STAGE_X(1, t + 3);
        WAIT_V(8); WAIT_L(0); BAR; MMA(1); BAR; SCHED;
    }
    LOAD_B(0); LOAD_A(0, 0); STAGE_Y(1, NT - 1);
    WAIT_V(8); WAIT_L(0); BAR; MMA(0); BAR; SCHED;
    LOAD_A(0, 1);
    WAIT_V(2); WAIT_L(0); BAR; MMA(1); BAR; SCHED;
    LOAD_B(1); LOAD_A(1, 0);
    WAIT_V(0); WAIT_L(0); BAR; MMA(0); BAR; SCHED;
    LOAD_A(1, 1);
    WAIT_L(0); BAR; MMA(1); BAR; SCHED;
    if (G == 0) BAR;
#undef STAGE_X
#undef STAGE_Y
#undef LOAD_A
#undef LOAD_B
#undef MMA

    if (!QKV) {
        char* gO = (char*)act + (size_t)(m0 + wq * 32 + fr) * (KACT * 2) + D_ * 2;
        const int pairoff = (32 * G + (fq & 1) * 16 + (fq >> 1) * 8) * 2;
#pragma unroll
        for (int h = 0; h < 2; ++h)
#pragma unroll
            for (int i = 0; i < 2; ++i) {
                char* rowp = gO + (size_t)(h * 128 + i * 16) * (KACT * 2);
#pragma unroll
                for (int j = 0; j < 3; ++j) {
                    const f32x4 a = acc[h][i][2 * j], b = acc[h][i][2 * j + 1];
                    unsigned ax = pack_bf16(fmaxf(a[0], 0.f), fmaxf(a[1], 0.f)), ay = pack_bf16(fmaxf(a[2], 0.f), fmaxf(a[3], 0.f));
                    unsigned bx = pack_bf16(fmaxf(b[0], 0.f), fmaxf(b[1], 0.f)), by = pack_bf16(fmaxf(b[2], 0.f), fmaxf(b[3], 0.f));
                    auto rx = __builtin_amdgcn_permlane16_swap(ax, bx, false, false);
                    auto ry = __builtin_amdgcn_permlane16_swap(ay, by, false, false);
                    u32x4 o = {rx[0], ry[0], rx[1], ry[1]};
                    *(u32x4*)(rowp + (cb0 + j * 64) * 2 + pairoff) = o;
                }
                const f32x4 e = accE[QKV ? 0 : h * 2 + i];
                u32x2 o2 = {pack_bf16(fmaxf(e[0], 0.f), fmaxf(e[1], 0.f)), pack_bf16(fmaxf(e[2], 0.f), fmaxf(e[3], 0.f))};
                *(u32x2*)(rowp + (cex + G * 16 + fq * 4) * 2) = o2;
            }
    } else {
        LAS unsigned char* hb = lds + G * ATT_HEAD;
#pragma unroll
        for (int h = 0; h < 2; ++h)
#pragma unroll
            for (int i = 0; i < 2; ++i) {
                const int row = h * 128 + wq * 32 + i * 16 + fr;
#pragma unroll
                for (int c = 0; c < 6; ++c) {
                    const f32x4 a = acc[h][i][c];
                    u32x2 o2 = {pack_bf16(a[0], a[1]), pack_bf16(a[2], a[3])};
                    const int base = (c >> 1) == 0 ? 0 : ((c >> 1) == 1 ? ATT_K + 16 * ATT_RS : ATT_V + 16 * ATT_RS);
                    *(LAS u32x2*)(hb + base + row * ATT_RS + (c & 1) * 32 + fq * 8) = o2;
                }
            }
        {
            const f32x4 a = accE[0];
            u32x2 o2 = {pack_bf16(a[0], a[1]), pack_bf16(a[2], a[3])};
            *(LAS u32x2*)(hb + ((wq >> 1) ? ATT_V : ATT_K) + fr * ATT_RS + (wq & 1) * 32 + fq * 8) = o2;
        }
        __syncthreads();
        const int lrow = wq * 64 + lane, srow = m0 + lrow;
        u32x4 qv[4];
#pragma unroll
        for (int c = 0; c < 4; ++c) qv[c] = *(LAS u32x4*)(hb + lrow * ATT_RS + c * 16);
        float lg[8];
        LAS unsigned char* kp = hb + ATT_K + (lrow + 2) * ATT_RS;
#pragma unroll
        for (int j = 0; j < 8; ++j) {
            float d0 = 0.f, d1 = 0.f;
#pragma unroll
            for (int c = 0; c < 4; ++c) {
                const u32x4 kv = *(LAS u32x4*)(kp + (14 - 2 * j) * ATT_RS + c * 16);
                d0 = dot2bf(qv[c][0], kv[0], d0); d1 = dot2bf(qv[c][1], kv[1], d1);
                d0 = dot2bf(qv[c][2], kv[2], d0); d1 = dot2bf(qv[c][3], kv[3], d1);
            }
            const float l = rbf(rbf(d0 + d1) * 0.17677669529663687f);
            lg[j] = (srow - 2 * j >= 0) ? l : -1e9f;
        }
        float mx = lg[0];
#pragma unroll
        for (int j = 1; j < 8; ++j) mx = fmaxf(mx, lg[j]);
        float sum = 0.f;
#pragma unroll
        for (int j = 0; j < 8; ++j) { lg[j] = __expf(lg[j] - mx); sum += lg[j]; }
        const float inv = 1.0f / sum;
        unsigned pp[4];
#pragma unroll
        for (int j = 0; j < 4; ++j) pp[j] = pack_bf16(lg[2 * j] * inv, lg[2 * j + 1] * inv);
        LAS unsigned char* vp = hb + ATT_V + (lrow + 2) * ATT_RS;
        char* gO = (char*)act + (size_t)srow * (KACT * 2) + (ct * 64 + G * 32) * 2;
#pragma unroll
        for (int c = 0; c < 4; ++c) {
            float o[8];
#pragma unroll
            for (int e = 0; e < 8; ++e) o[e] = 0.f;
#pragma unroll
            for (int j = 0; j < 4; ++j) {
                const u32x4 va = *(LAS u32x4*)(vp + (14 - 4 * j) * ATT_RS + c * 16);
                const u32x4 vb = *(LAS u32x4*)(vp + (12 - 4 * j) * ATT_RS + c * 16);
#pragma unroll
                for (int e = 0; e < 4; ++e) {
                    o[2 * e] = dot2bf(__builtin_amdgcn_perm(vb[e], va[e], 0x05040100u), pp[j], o[2 * e]);
                    o[2 * e + 1] = dot2bf(__builtin_amdgcn_perm(vb[e], va[e], 0x07060302u), pp[j], o[2 * e + 1]);
                }
            }
            u32x4 ov = {pack_bf16(o[0], o[1]), pack_bf16(o[2], o[3]), pack_bf16(o[4], o[5]), pack_bf16(o[6], o[7])};
            *(u32x4*)(gO + c * 16) = ov;
        }
    }
}

__global__ __launch_bounds__(512, 2) void gemm1_kernel(const __hip_bfloat16* __restrict__ xn, const __hip_bfloat16* __restrict__ wqkv,
                                                       const __hip_bfloat16* __restrict__ w1, __hip_bfloat16* __restrict__ act) {
    extern __shared__ __attribute__((aligned(16))) unsigned char smem[];
    LAS unsigned char* lds = (LAS unsigned char*)smem;
    const int xcd = blockIdx.x & 7, j = blockIdx.x >> 3;
    const int ct = xcd * 2 + (j & 1), mt = j >> 2;
    if ((j >> 1) & 1) gemm1_body<true>(lds, xn, wqkv, act, mt, ct);
    else gemm1_body<false>(lds, xn, w1, act, mt, ct);
}

constexpr int G2_NS = 5, G2_STAGE = 32768, G2_LDS = G2_NS * G2_STAGE, G2_NT = KACT / 64 / 2;

__global__ __launch_bounds__(512, 2) void gemm2_kernel(const __hip_bfloat16* __restrict__ act, const __hip_bfloat16* __restrict__ wo,
                                                       const __hip_bfloat16* __restrict__ w2, const __hip_bfloat16* __restrict__ x,
                                                       __hip_bfloat16* __restrict__ out, float* __restrict__ xbuf, unsigned* __restrict__ flags) {
    extern __shared__ __attribute__((aligned(16))) unsigned char smem[];
    LAS unsigned char* lds = (LAS unsigned char*)smem;
    const int tid = threadIdx.x, wid = __builtin_amdgcn_readfirstlane(tid >> 6), lane = tid & 63;
    const int G = wid >> 2, wq = wid & 3, wm = wq >> 1, wn = wq & 1, fr = lane & 15, fq = lane >> 4;
    const int xcd = blockIdx.x & 7, jb = blockIdx.x >> 3;
    const int sp = xcd & 1, mt = (xcd >> 1) * 4 + (jb & 3), nt = jb >> 2;
    const int m0 = mt * 128, n0 = nt * 128, kt0 = sp * G2_NT;

    const unsigned voffA = (unsigned)((8 * wq + (lane >> 3)) * (KACT * 2) + (((lane & 7) ^ ((4 * wq + (lane >> 4)) & 7)) << 4));
    const unsigned voffB = (unsigned)((8 * wq + (lane >> 3)) * (D_ * 2) + (((lane & 7) ^ ((((lane >> 4) & 1) + 2 * (wq & 1)) << 1)) << 4));
    const char* gA = (const char*)act + (size_t)m0 * (KACT * 2) + (size_t)kt0 * 128;
    const unsigned ldsw = (unsigned)(uintptr_t)lds + wq * 1024;
    auto stage = [&](int t, int slot) {
        const int kt = kt0 + t;
        const char* gB = (kt < 16 ? (const char*)wo + (size_t)kt * (64 * D_ * 2) : (const char*)w2 + (size_t)(kt - 16) * (64 * D_ * 2)) + n0 * 2;
        const unsigned d = ldsw + slot * G2_STAGE;
#pragma unroll
        for (int u = 0; u < 4; ++u) glds_s(gB + (size_t)(u & 1) * (32 * D_ * 2) + (u >> 1) * 128, voffB, d + 16384 + (u >> 1) * 8192 + (u & 1) * 4096);
#pragma unroll
        for (int u = 0; u < 4; ++u) glds_s(gA + (size_t)t * 128 + (size_t)u * (32 * KACT * 2), voffA, d + u * 4096);
    };
    const int q4 = (lane & 15) >> 2, p4 = lane & 3;
    const int aoff = (wm * 64 + fr) * 128 + ((fq ^ ((fr >> 1) & 7)) << 4);
    const int boff = 16384 + wn * 8192 + fq * 1024 + q4 * 128 + ((((q4 >> 1) & 1) + 2 * (fq & 1)) << 5) + p4 * 8;

    f32x4 acc[4][4];
#pragma unroll
    for (int i = 0; i < 4; ++i)
#pragma unroll
        for (int c = 0; c < 4; ++c) acc[i][c] = (f32x4){0.f, 0.f, 0.f, 0.f};
    bf16x8 Af[4][2], Bf[4][2];

    stage(G, G); stage(G + 2, G + 2);
    if (G == 1) BAR;
    WAIT_V(8); BAR;
    int slot = G, sslot = (G + 4) % G2_NS;
    for (int t = G; t < G2_NT; t += 2) {
        LAS unsigned char* sb = lds + slot * G2_STAGE;
#pragma unroll
        for (int k = 0; k < 2; ++k) {
#pragma unroll
            for (int c = 0; c < 4; ++c) Bf[c][k] = lds_tr(sb + (boff ^ (c << 5)) + k * 4096, 512);
#pragma unroll
            for (int i = 0; i < 4; ++i) Af[i][k] = lds_b128(sb + (aoff ^ (k << 6)) + i * 2048);
        }
        const bool more = t + 4 < G2_NT;
        if (more) stage(t + 4, sslot);
        WAIT_L(0); BAR;
        __builtin_amdgcn_s_setprio(1);
#pragma unroll
        for (int k = 0; k < 2; ++k)
#pragma unroll
            for (int c = 0; c < 4; ++c)
#pragma unroll
                for (int i = 0; i < 4; ++i) acc[i][c] = mfma16(Bf[c][k], Af[i][k], acc[i][c]);
        __builtin_amdgcn_s_setprio(0);
        if (more) WAIT_V(8); else WAIT_V(0);
        BAR; SCHED;
        slot += 2; if (slot >= G2_NS) slot -= G2_NS;
        sslot += 2; if (sslot >= G2_NS) sslot -= G2_NS;
    }
    if (G == 0) BAR;

    const int frow = m0 + wm * 64 + (2 * G + sp) * 16 + fr;
    const size_t fcol = (size_t)n0 + wn * 64 + fq * 4;
    u32x2 xr[4];
#pragma unroll
    for (int c = 0; c < 4; ++c) xr[c] = *(const u32x2*)(x + (size_t)frow * D_ + fcol + c * 16);
    f32x4 keep[2][4];
    {
        LAS f32x4* wr = (LAS f32x4*)(lds + wid * 8192) + lane;
        LAS f32x4* rd = (LAS f32x4*)(lds + (wid ^ 4) * 8192) + lane;
#pragma unroll
        for (int u = 0; u < 2; ++u)
#pragma unroll
            for (int c = 0; c < 4; ++c) {
                wr[(u * 4 + c) * 64] = sel4(G, acc[u][c], acc[2 + u][c]);
                keep[u][c] = sel4(G, acc[2 + u][c], acc[u][c]);
            }
        __syncthreads();
#pragma unroll
        for (int u = 0; u < 2; ++u)
#pragma unroll
            for (int c = 0; c < 4; ++c) keep[u][c] += rd[(u * 4 + c) * 64];
    }
    const int pair = (mt * 8 + nt) * 2;
    const __amdgpu_buffer_rsrc_t rs = __builtin_amdgcn_make_buffer_rsrc((void*)xbuf, 0, 256 * 32768, 0x00020000);
    const int xo = wid * 4096 + lane * 16;
#pragma unroll
    for (int c = 0; c < 4; ++c) {
        const f32x4 v = sel4(sp, keep[0][c], keep[1][c]);
        __builtin_amdgcn_raw_buffer_store_b128(__builtin_bit_cast(u32x4, v), rs, (pair + sp) * 32768 + xo + c * 1024, 0, 16);
    }
    WAIT_V(0);
    __syncthreads();
    if (wid == 0) {
        if (lane == 0) __hip_atomic_store((GAS unsigned*)(flags + pair + sp), 1u, __ATOMIC_RELAXED, __HIP_MEMORY_SCOPE_AGENT);
        for (int spin = 0; spin < (1 << 22); ++spin) {
            const unsigned f = __hip_atomic_load((GAS unsigned*)(flags + pair + (sp ^ 1)), __ATOMIC_RELAXED, __HIP_MEMORY_SCOPE_AGENT);
            if (__builtin_amdgcn_readfirstlane(f) == 1u) break;
            __builtin_amdgcn_s_sleep(1);
        }
        __builtin_amdgcn_fence(__ATOMIC_ACQUIRE, "wavefront");
    }
    __syncthreads();
    unsigned pk[4][2];
#pragma unroll
    for (int c = 0; c < 4; ++c) {
        const f32x4 r = __builtin_bit_cast(f32x4, __builtin_amdgcn_raw_buffer_load_b128(rs, (pair + (sp ^ 1)) * 32768 + xo + c * 1024, 0, 16));
        const f32x4 v = sel4(sp, keep[1][c], keep[0][c]) + r;
        pk[c][0] = pack_bf16(v[0] + bf_lo(xr[c][0]), v[1] + bf_hi(xr[c][0]));
        pk[c][1] = pack_bf16(v[2] + bf_lo(xr[c][1]), v[3] + bf_hi(xr[c][1]));
    }
    char* gO = (char*)out + (size_t)frow * (D_ * 2) + (n0 + wn * 64 + (fq & 1) * 16 + (fq >> 1) * 8) * 2;
#pragma unroll
    for (int j = 0; j < 2; ++j) {
        auto rx = __builtin_amdgcn_permlane16_swap(pk[2 * j][0], pk[2 * j + 1][0], false, false);
        auto ry = __builtin_amdgcn_permlane16_swap(pk[2 * j][1], pk[2 * j + 1][1], false, false);
        u32x4 o = {rx[0], ry[0], rx[1], ry[1]};
        *(u32x4*)(gO + j * 64) = o;
    }
}

extern "C" void kernel_launch(void* const* d_in, const int* in_sizes, int n_in, void* d_out, int out_size, void* d_ws,
                              size_t ws_size, hipStream_t stream) {
    const __hip_bfloat16* x = (const __hip_bfloat16*)d_in[0];
    const __hip_bfloat16* wqkv = (const __hip_bfloat16*)d_in[1];
    const __hip_bfloat16* wo = (const __hip_bfloat16*)d_in[2];
    const __hip_bfloat16* w1 = (const __hip_bfloat16*)d_in[4];
    const __hip_bfloat16* w2 = (const __hip_bfloat16*)d_in[5];
    char* ws = (char*)d_ws;
    __hip_bfloat16* xn = (__hip_bfloat16*)ws;
    __hip_bfloat16* act = (__hip_bfloat16*)(ws + (4u << 20));
    float* xbuf = (float*)(ws + (24u << 20));
    unsigned* flags = (unsigned*)(ws + (32u << 20));
    (void)hipFuncSetAttribute((const void*)gemm1_kernel, hipFuncAttributeMaxDynamicSharedMemorySize, G1_LDS);
    (void)hipFuncSetAttribute((const void*)gemm2_kernel, hipFuncAttributeMaxDynamicSharedMemorySize, G2_LDS);
    ln_kernel<<<S_ / 4, 256, 0, stream>>>(x, xn, flags);
    gemm1_kernel<<<256, 512, G1_LDS, stream>>>(xn, wqkv, w1, act);
    gemm2_kernel<<<256, 512, G2_LDS, stream>>>(act, wo, w2, x, (__hip_bfloat16*)d_out, xbuf, flags);
}
```

```cpp
#include <hip/hip_runtime.h>
#include <hip/hip_bf16.h>
#include <stdint.h>

#define LAS __attribute__((address_space(3)))
#define GAS __attribute__((address_space(1)))
typedef __attribute__((ext_vector_type(8))) short bf16x8;
typedef __attribute__((ext_vector_type(4))) short s16x4;
typedef __attribute__((ext_vector_type(4))) float f32x4;
typedef __attribute__((ext_vector_type(2))) float f32x2;
typedef __attribute__((ext_vector_type(2))) __bf16 bf16x2_t;
typedef __attribute__((ext_vector_type(4))) unsigned u32x4;
typedef __attribute__((ext_vector_type(2))) unsigned u32x2;

constexpr int S_ = 2048, D_ = 1024, DFF = 3584, KACT = D_ + DFF;

#define WAIT_V(n) asm volatile("s_waitcnt vmcnt(" #n ")" ::: "memory")
#define WAIT_L(n) asm volatile("s_waitcnt lgkmcnt(" #n ")" ::: "memory")
#define BAR do { __builtin_amdgcn_s_barrier(); asm volatile("" ::: "memory"); } while (0)
#define SCHED __builtin_amdgcn_sched_barrier(0)
#define STAMP(k, i)
#define GLDS(src, dst) __builtin_amdgcn_global_load_lds((const unsigned*)(src), (LAS unsigned*)(dst), 16, 0, 0)

__device__ __forceinline__ void glds_s(const void* sbase, unsigned voff, unsigned ldsaddr) {
    asm volatile("s_mov_b32 m0, %2\n\ts_nop 0\n\tglobal_load_lds_dwordx4 %0, %1" ::"v"(voff), "s"(sbase), "s"(ldsaddr) : "memory");
}
__device__ __forceinline__ void glds4_s(const void* sbase, unsigned voff, unsigned ldsaddr) {
    asm volatile("s_mov_b32 m0, %2\n\ts_nop 0\n\tglobal_load_lds_dword %0, %1" ::"v"(voff), "s"(sbase), "s"(ldsaddr) : "memory");
}
__device__ __forceinline__ unsigned pack_bf16(float a, float b) {
    f32x2 v = {a, b};
    bf16x2_t r = __builtin_convertvector(v, bf16x2_t);
    return __builtin_bit_cast(unsigned, r);
}
__device__ __forceinline__ float bf_lo(unsigned u) { return __builtin_bit_cast(float, u << 16); }
__device__ __forceinline__ float bf_hi(unsigned u) { return __builtin_bit_cast(float, u & 0xffff0000u); }
__device__ __forceinline__ float rbf(float a) { return bf_lo(pack_bf16(a, a)); }
__device__ __forceinline__ bf16x8 lds_b128(LAS unsigned char* p) { return *(LAS bf16x8*)p; }
__device__ __forceinline__ bf16x8 lds_tr(LAS unsigned char* p, int hi_off) {
    s16x4 lo = __builtin_amdgcn_ds_read_tr16_b64_v4i16((LAS s16x4*)p);
    s16x4 hi = __builtin_amdgcn_ds_read_tr16_b64_v4i16((LAS s16x4*)(p + hi_off));
    return __builtin_shufflevector(lo, hi, 0, 1, 2, 3, 4, 5, 6, 7);
}
__device__ __forceinline__ f32x4 mfma16(bf16x8 a, bf16x8 b, f32x4 c) {
    return __builtin_amdgcn_mfma_f32_16x16x32_bf16(a, b, c, 0, 0, 0);
}
__device__ __forceinline__ f32x4 sel4(bool c, f32x4 a, f32x4 b) {
    f32x4 r;
#pragma unroll
    for (int e = 0; e < 4; ++e) r[e] = c ? a[e] : b[e];
    return r;
}
__device__ __forceinline__ float dot2bf(unsigned a, unsigned b, float c) {
    return __builtin_amdgcn_fdot2_f32_bf16(__builtin_bit_cast(bf16x2_t, a), __builtin_bit_cast(bf16x2_t, b), c, false);
}

__global__ __launch_bounds__(256) void ln_kernel(const __hip_bfloat16* __restrict__ x, __hip_bfloat16* __restrict__ xn,
                                                 unsigned* __restrict__ flags) {
    const int lane = threadIdx.x & 63, row = blockIdx.x * 4 + (threadIdx.x >> 6);
    if (blockIdx.x == 0) flags[threadIdx.x] = 0u;
    const u32x4* src = (const u32x4*)(x + (size_t)row * D_);
    u32x4 v0 = src[lane], v1 = src[64 + lane];
    float f[16];
#pragma unroll
    for (int i = 0; i < 4; ++i) { f[2 * i] = bf_lo(v0[i]); f[2 * i + 1] = bf_hi(v0[i]); f[8 + 2 * i] = bf_lo(v1[i]); f[9 + 2 * i] = bf_hi(v1[i]); }
    float s = 0.f;
#pragma unroll
    for (int i = 0; i < 16; ++i) s += f[i];
#pragma unroll
    for (int o = 32; o >= 1; o >>= 1) s += __shfl_xor(s, o);
    const float mean = s * (1.0f / D_);
    float q = 0.f;
#pragma unroll
    for (int i = 0; i < 16; ++i) { const float d = f[i] - mean; q += d * d; }
#pragma unroll
    for (int o = 32; o >= 1; o >>= 1) q += __shfl_xor(q, o);
    const float mb = rbf(mean);
    const float sd = rbf(sqrtf(rbf(rbf(q * (1.0f / D_)) + 1e-5f)));
    u32x4 o0, o1;
#pragma unroll
    for (int i = 0; i < 4; ++i) {
        o0[i] = pack_bf16(rbf(f[2 * i] - mb) / sd, rbf(f[2 * i + 1] - mb) / sd);
        o1[i] = pack_bf16(rbf(f[8 + 2 * i] - mb) / sd, rbf(f[9 + 2 * i] - mb) / sd);
    }
    u32x4* dst = (u32x4*)(xn + (size_t)row * D_);
    dst[lane] = o0; dst[64 + lane] = o1;
}

constexpr int G1_ABUF = 34816, G1_BOFF = 3 * G1_ABUF, G1_BBUF = 14336, G1_PFOFF = G1_BOFF + 4 * G1_BBUF, G1_LDS = G1_PFOFF + 2048;
constexpr int ATT_HEAD = 64000, ATT_K = 20480, ATT_V = 20480 + 21760, ATT_RS = 80, H_RS = 464;

template <bool QKV>
__device__ __forceinline__ void gemm1_body(LAS unsigned char* lds, const __hip_bfloat16* __restrict__ xn,
                                           const __hip_bfloat16* __restrict__ w, __hip_bfloat16* __restrict__ act,
                                           const char* __restrict__ pf0, const char* __restrict__ pf1, const int mt, const int ct) {
    constexpr int LDB = QKV ? 3 * D_ : DFF;
    const int tid = threadIdx.x, wid = __builtin_amdgcn_readfirstlane(tid >> 6), lane = tid & 63;
    const int G = wid >> 2, wq = wid & 3, fr = lane & 15, fq = lane >> 4;
    const int m0 = mt * 256;
    int cb0, cstep, cex;
    if (QKV) { cb0 = ct * 64; cstep = D_; cex = 0; }
    else { const int n0 = ct * 224; cb0 = n0 + ((ct & 1) ? 32 : 0); cstep = 64; cex = (ct & 1) ? n0 : n0 + 192; }

    const unsigned lds0 = (unsigned)(uintptr_t)lds;
    const unsigned voffA = (unsigned)((8 * wid + (lane >> 3)) * (D_ * 2) + (((lane & 7) ^ ((4 * wid + (lane >> 4)) & 7)) << 4));
    const unsigned voffB = (unsigned)((8 * wq + (lane >> 3)) * (LDB * 2) + (((lane & 7) ^ ((((lane >> 4) & 1) + 2 * (wq & 1)) << 1)) << 4));
    unsigned voff2 = voffB;
    if (G) {
        if (QKV) voff2 = (unsigned)((4 * wq + ((lane >> 3) & 3)) * (D_ * 2) + (((lane & 7) ^ ((2 * wq + ((lane >> 4) & 1)) & 7)) << 4));
        else voff2 = (unsigned)((8 * wq + ((lane >> 2) & 7)) * (LDB * 2) + ((((lane >> 1) & 1) ^ (wq & 1)) << 5) + ((lane & 1) << 4));
    }
    const char* gA = (const char*)xn + (size_t)m0 * (D_ * 2);
    const char* gB1 = (const char*)w + (size_t)(cb0 + G * cstep) * 2;
    const char* gB2 = G ? (QKV ? (const char*)xn + (size_t)(m0 >= 16 ? m0 - 16 : 0) * (D_ * 2) : (const char*)w + (size_t)cex * 2)
                        : (const char*)w + (size_t)(cb0 + 2 * cstep) * 2;
    constexpr size_t KSB = (size_t)32 * LDB * 2;
    const unsigned dB1 = lds0 + G1_BOFF + G * 4096 + wq * 1024;
    const unsigned dB2 = G ? (QKV ? lds0 + 32768 + wq * 512 : lds0 + G1_BOFF + 12288 + wq * 512) : lds0 + G1_BOFF + 8192 + wq * 1024;
    const bool halo = QKV && G;
    const int lim2 = G ? 32 : 64;
#define STAGE_A(t, kh, ab) do { \
        glds_s(gA + (size_t)(t) * 128 + (size_t)(2 * (kh)) * (64 * D_ * 2), voffA, lds0 + (ab) + (kh) * 16384 + wid * 1024); \
        glds_s(gA + (size_t)(t) * 128 + (size_t)(2 * (kh) + 1) * (64 * D_ * 2), voffA, lds0 + (ab) + (kh) * 16384 + 8192 + wid * 1024); } while (0)
#define STAGE_B(h, s, t, ab) do { \
        glds_s(gB1 + (size_t)(h) * KSB, voffB, dB1 + (s) * G1_BBUF); \
        if (lane < lim2) glds_s(gB2 + (halo ? (size_t)(t) * 128 : (size_t)(h) * KSB), voff2, dB2 + (halo ? (unsigned)(ab) : (unsigned)((s) * G1_BBUF))); } while (0)

    const int q4 = (lane & 15) >> 2, p4 = lane & 3;
    const int aswz = (fq ^ ((fr >> 1) & 7)) << 4;
    const int oA = (wq * 64 + fr) * 128 + aswz, oH = 32768 + fr * 128 + aswz;
    const int bsw = ((q4 >> 1) & 1) + 2 * (fq & 1);
    const int bbase = G1_BOFF + fq * 1024 + q4 * 128 + p4 * 8;
    LAS unsigned char* pB[2] = {lds + bbase + (((2 * G) ^ bsw) << 5), lds + bbase + (((2 * G + 1) ^ bsw) << 5)};
    LAS unsigned char* pEB = QKV ? lds + bbase + (1 + (wq >> 1)) * 4096 + (((2 * G + (wq & 1)) ^ bsw) << 5)
                                 : lds + G1_BOFF + 12288 + fq * 512 + q4 * 64 + ((G ^ (fq & 1)) << 5) + p4 * 8;

    f32x4 acc[4][6], accE[QKV ? 1 : 4];
#pragma unroll
    for (int i = 0; i < 4; ++i)
#pragma unroll
        for (int c = 0; c < 6; ++c) acc[i][c] = (f32x4){0.f, 0.f, 0.f, 0.f};
#pragma unroll
    for (int i = 0; i < (QKV ? 1 : 4); ++i) accE[i] = (f32x4){0.f, 0.f, 0.f, 0.f};
    bf16x8 Af[4], Bf[6], Be, Ae;

#define LOADF(s, kh, ab) do { \
        _Pragma("unroll") for (int _c = 0; _c < 6; ++_c) Bf[_c] = lds_tr(pB[_c & 1] + (s) * G1_BBUF + (_c >> 1) * 4096, 512); \
        Be = lds_tr(pEB + (s) * G1_BBUF, QKV ? 512 : 256); \
        _Pragma("unroll") for (int _i = 0; _i < 4; ++_i) Af[_i] = lds_b128(lds + (ab) + (oA ^ ((kh) << 6)) + _i * 2048); \
        if (QKV) Ae = lds_b128(lds + (ab) + (oH ^ ((kh) << 6))); \
        SCHED; } while (0)
#define MMA() do { WAIT_L(0); BAR; __builtin_amdgcn_s_setprio(1); \
        _Pragma("unroll") for (int _c = 0; _c < 6; ++_c) _Pragma("unroll") for (int _i = 0; _i < 4; ++_i) \
            acc[_i][_c] = mfma16(Bf[_c], Af[_i], acc[_i][_c]); \
        if (QKV) accE[0] = mfma16(Be, Ae, accE[0]); \
        else { _Pragma("unroll") for (int _i = 0; _i < 4; ++_i) accE[QKV ? 0 : _i] = mfma16(Be, Af[_i], accE[QKV ? 0 : _i]); } \
        __builtin_amdgcn_s_setprio(0); BAR; SCHED; } while (0)

    STAMP(0, 0);
    STAGE_A(0, 0, 0); STAGE_A(0, 1, 0); STAGE_B(0, 0, 0, 0);
    STAGE_A(1, 0, G1_ABUF); STAGE_B(1, 1, 1, G1_ABUF);
    STAGE_A(1, 1, G1_ABUF); STAGE_B(2, 2, 1, G1_ABUF);
    WAIT_V(8); BAR;
    if (G == 1) BAR;
    STAMP(0, 1);
    int a0 = 0, a1 = G1_ABUF, a2 = 2 * G1_ABUF;
    for (int t = 0; t < 14; t += 2) {
        const int h = 2 * t;
        LOADF(0, 0, a0); STAGE_A(t + 2, 0, a2); STAGE_B(h + 3, 3, t + 2, a2); WAIT_V(8); MMA();
        LOADF(1, 1, a0); STAGE_A(t + 2, 1, a2); STAGE_B(h + 4, 0, t + 2, a2); WAIT_V(8); MMA();
        LOADF(2, 0, a1); STAGE_A(t + 3, 0, a0); STAGE_B(h + 5, 1, t + 3, a0); WAIT_V(8); MMA();
        LOADF(3, 1, a1); STAGE_A(t + 3, 1, a0); STAGE_B(h + 6, 2, t + 3, a0); WAIT_V(8); MMA();
        const int a = a0; a0 = a2; a2 = a1; a1 = a;
    }
    LOADF(0, 0, a0); STAGE_B(31, 3, 15, a1); WAIT_V(6); MMA();
    LOADF(1, 1, a0);
    glds4_s(pf0, tid * 64, lds0 + G1_PFOFF + wid * 256); glds4_s(pf1, lane * 64, lds0 + G1_PFOFF + wid * 256);
    WAIT_V(4); MMA();
    LOADF(2, 0, a1); WAIT_V(2); MMA();
    LOADF(3, 1, a1); MMA();
    if (G == 0) BAR;
    STAMP(0, 2);
#undef STAGE_A
#undef STAGE_B
#undef LOADF
#undef MMA

    if (!QKV) {
        const int n0 = ct * 224;
        LAS unsigned char* wp = lds + (wq * 64 + fr) * H_RS + fq * 8;
#pragma unroll
        for (int i = 0; i < 4; ++i) {
#pragma unroll
            for (int c = 0; c < 7; ++c) {
                const f32x4 a = c < 6 ? acc[i][c < 6 ? c : 0] : accE[QKV ? 0 : i];
                const int col = c < 6 ? (cb0 - n0) + (c >> 1) * 64 + (2 * G + (c & 1)) * 16 : (cex - n0) + G * 16;
                u32x2 o2 = {pack_bf16(fmaxf(a[0], 0.f), fmaxf(a[1], 0.f)), pack_bf16(fmaxf(a[2], 0.f), fmaxf(a[3], 0.f))};
                *(LAS u32x2*)(wp + i * 16 * H_RS + col * 2) = o2;
            }
        }
        WAIT_L(0); BAR;
        STAMP(0, 3);
        char* gO = (char*)act + (size_t)m0 * (KACT * 2) + (D_ + n0) * 2;
#pragma unroll
        for (int it = 0; it < 14; ++it) {
            const int idx = it * 512 + tid, row = idx / 28, ch = idx - row * 28;
            const u32x4 v = *(LAS u32x4*)(lds + row * H_RS + ch * 16);
            *(u32x4*)(gO + (size_t)row * (KACT * 2) + ch * 16) = v;
        }
    } else {
        LAS unsigned char* hb = lds + G * ATT_HEAD;
#pragma unroll
        for (int i = 0; i < 4; ++i) {
            const int row = wq * 64 + i * 16 + fr;
#pragma unroll
            for (int c = 0; c < 6; ++c) {
                const f32x4 a = acc[i][c];
                u32x2 o2 = {pack_bf16(a[0], a[1]), pack_bf16(a[2], a[3])};
                const int base = (c >> 1) == 0 ? 0 : ((c >> 1) == 1 ? ATT_K + 16 * ATT_RS : ATT_V + 16 * ATT_RS);
                *(LAS u32x2*)(hb + base + row * ATT_RS + (c & 1) * 32 + fq * 8) = o2;
            }
        }
        {
            const f32x4 a = accE[0];
            u32x2 o2 = {pack_bf16(a[0], a[1]), pack_bf16(a[2], a[3])};
            *(LAS u32x2*)(hb + ((wq >> 1) ? ATT_V : ATT_K) + fr * ATT_RS + (wq & 1) * 32 + fq * 8) = o2;
        }
        WAIT_L(0); BAR;
        STAMP(0, 3);
        const int lrow = wq * 64 + lane, srow = m0 + lrow;
        u32x4 qv[4];
#pragma unroll
        for (int c = 0; c < 4; ++c) qv[c] = *(LAS u32x4*)(hb + lrow * ATT_RS + c * 16);
        float lg[8];
        LAS unsigned char* kp = hb + ATT_K + (lrow + 2) * ATT_RS;
#pragma unroll
        for (int j = 0; j < 8; ++j) {
            float d0 = 0.f, d1 = 0.f;
#pragma unroll
            for (int c = 0; c < 4; ++c) {
                const u32x4 kv = *(LAS u32x4*)(kp + (14 - 2 * j) * ATT_RS + c * 16);
                d0 = dot2bf(qv[c][0], kv[0], d0); d1 = dot2bf(qv[c][1], kv[1], d1);
                d0 = dot2bf(qv[c][2], kv[2], d0); d1 = dot2bf(qv[c][3], kv[3], d1);
            }
            const float l = rbf(rbf(d0 + d1) * 0.17677669529663687f);
            lg[j] = (srow - 2 * j >= 0) ? l : -1e9f;
        }
        float mx = lg[0];
#pragma unroll
        for (int j = 1; j < 8; ++j) mx = fmaxf(mx, lg[j]);
        float sum = 0.f;
#pragma unroll
        for (int j = 0; j < 8; ++j) { lg[j] = __expf(lg[j] - mx); sum += lg[j]; }
        const float inv = 1.0f / sum;
        unsigned pp[4];
#pragma unroll
        for (int j = 0; j < 4; ++j) pp[j] = pack_bf16(lg[2 * j] * inv, lg[2 * j + 1] * inv);
        LAS unsigned char* vp = hb + ATT_V + (lrow + 2) * ATT_RS;
#pragma unroll
        for (int c = 0; c < 4; ++c) {
            float o[8];
#pragma unroll
            for (int e = 0; e < 8; ++e) o[e] = 0.f;
#pragma unroll
            for (int j = 0; j < 4; ++j) {
                const u32x4 va = *(LAS u32x4*)(vp + (14 - 4 * j) * ATT_RS + c * 16);
                const u32x4 vb = *(LAS u32x4*)(vp + (12 - 4 * j) * ATT_RS + c * 16);
#pragma unroll
                for (int e = 0; e < 4; ++e) {
                    o[2 * e] = dot2bf(__builtin_amdgcn_perm(vb[e], va[e], 0x05040100u), pp[j], o[2 * e]);
                    o[2 * e + 1] = dot2bf(__builtin_amdgcn_perm(vb[e], va[e], 0x07060302u), pp[j], o[2 * e + 1]);
                }
            }
            u32x4 ov = {pack_bf16(o[0], o[1]), pack_bf16(o[2], o[3]), pack_bf16(o[4], o[5]), pack_bf16(o[6], o[7])};
            *(LAS u32x4*)(hb + lrow * ATT_RS + c * 16) = ov;
        }
        char* gO = (char*)act + (size_t)(m0 + wq * 64 + (lane >> 2)) * (KACT * 2) + (ct * 64 + G * 32) * 2 + (lane & 3) * 16;
#pragma unroll
        for (int i = 0; i < 4; ++i) {
            const u32x4 v = *(LAS u32x4*)(hb + (wq * 64 + i * 16 + (lane >> 2)) * ATT_RS + (lane & 3) * 16);
            *(u32x4*)(gO + (size_t)(i * 16) * (KACT * 2)) = v;
        }
    }
    STAMP(0, 4);
    STAMP(0, 5);
}

__global__ __launch_bounds__(512, 2) void gemm1_kernel(const __hip_bfloat16* __restrict__ xn, const __hip_bfloat16* __restrict__ wqkv,
                                                       const __hip_bfloat16* __restrict__ w1, __hip_bfloat16* __restrict__ act,
                                                       const __hip_bfloat16* __restrict__ wo, const __hip_bfloat16* __restrict__ w2) {
    extern __shared__ __attribute__((aligned(16))) unsigned char smem[];
    LAS unsigned char* lds = (LAS unsigned char*)smem;
    const int xcd = blockIdx.x & 7, j = blockIdx.x >> 3;
    const int ct = xcd * 2 + (j & 1), mt = j >> 2;
    const int wid = __builtin_amdgcn_readfirstlane(threadIdx.x >> 6);
    const char* pf0 = blockIdx.x < 224 ? (const char*)w2 + (size_t)blockIdx.x * 32768 : (const char*)wo + (size_t)(blockIdx.x - 224) * 32768;
    const char* pf1 = (const char*)wo + (1u << 20) + (size_t)((blockIdx.x * 8 + wid) & 255) * 4096;
    if ((j >> 1) & 1) gemm1_body<true>(lds, xn, wqkv, act, pf0, pf1, mt, ct);
    else gemm1_body<false>(lds, xn, w1, act, pf0, pf1, mt, ct);
}

constexpr int G2_NS = 5, G2_STAGE = 32768, G2_LDS = G2_NS * G2_STAGE, G2_NT = KACT / 64 / 2;

__global__ __launch_bounds__(512, 2) void gemm2_kernel(const __hip_bfloat16* __restrict__ act, const __hip_bfloat16* __restrict__ wo,
                                                       const __hip_bfloat16* __restrict__ w2, const __hip_bfloat16* __restrict__ x,
                                                       __hip_bfloat16* __restrict__ out, float* __restrict__ xbuf, unsigned* __restrict__ flags) {
    extern __shared__ __attribute__((aligned(16))) unsigned char smem[];
    LAS unsigned char* lds = (LAS unsigned char*)smem;
    const int tid = threadIdx.x, wid = __builtin_amdgcn_readfirstlane(tid >> 6), lane = tid & 63;
    const int G = wid >> 2, wq = wid & 3, wm = wq >> 1, wn = wq & 1, fr = lane & 15, fq = lane >> 4;
    const int xcd = blockIdx.x & 7, jb = blockIdx.x >> 3;
    const int sp = xcd & 1, mt = (xcd >> 1) * 4 + (jb & 3), nt = jb >> 2;
    const int m0 = mt * 128, n0 = nt * 128, kt0 = sp * G2_NT;

    const unsigned voffA = (unsigned)((8 * wid + (lane >> 3)) * (KACT * 2) + (((lane & 7) ^ ((4 * wid + (lane >> 4)) & 7)) << 4));
    const unsigned voffB = (unsigned)((8 * wid + (lane >> 3)) * (D_ * 2) + (((lane & 7) ^ ((((lane >> 4) & 1) + 2 * (wid & 1)) << 1)) << 4));
    const char* gA = (const char*)act + (size_t)m0 * (KACT * 2) + (size_t)kt0 * 128;
    const unsigned ldsw = (unsigned)(uintptr_t)lds + wid * 1024;
    auto bsrc = [&](int t) {
        const int kt = kt0 + t;
        return (kt < 16 ? (const char*)wo + (size_t)kt * (64 * D_ * 2) : (const char*)w2 + (size_t)(kt - 16) * (64 * D_ * 2)) + n0 * 2;
    };
#define ST_B(gb, slot, u) glds_s((gb) + (u) * 128, voffB, ldsw + (slot) * G2_STAGE + 16384 + (u) * 8192)
#define ST_A(t, slot, u) glds_s(gA + (size_t)(t) * 128 + (size_t)(u) * (64 * KACT * 2), voffA, ldsw + (slot) * G2_STAGE + (u) * 8192)
    const int q4 = (lane & 15) >> 2, p4 = lane & 3;
    const int aoff = (wm * 64 + fr) * 128 + ((fq ^ ((fr >> 1) & 7)) << 4);
    const int boff = 16384 + wn * 8192 + fq * 1024 + q4 * 128 + ((((q4 >> 1) & 1) + 2 * (fq & 1)) << 5) + p4 * 8;

    STAMP(1, 0);
#pragma unroll
    for (int t = 0; t < 4; ++t) { const char* gb = bsrc(t); ST_B(gb, t, 0); ST_B(gb, t, 1); ST_A(t, t, 0); ST_A(t, t, 1); }
    const int frow = m0 + wm * 64 + (2 * G + sp) * 16 + fr;

    f32x4 acc[4][4];
#pragma unroll
    for (int i = 0; i < 4; ++i)
#pragma unroll
        for (int c = 0; c < 4; ++c) acc[i][c] = (f32x4){0.f, 0.f, 0.f, 0.f};
    bf16x8 Af[4][2], Bf[4][2];

    WAIT_V(12); BAR;
    int ss = 4;
    if (G == 1) {
        const char* gb = bsrc(4); ST_B(gb, 4, 0); ST_B(gb, 4, 1); ST_A(4, 4, 0); ST_A(4, 4, 1); ss = 0;
        WAIT_V(12); BAR;
    }
    STAMP(1, 1);
    int slot = G;
    for (int t = G; t < G2_NT; t += 2) {
        LAS unsigned char* sb = lds + slot * G2_STAGE;
#pragma unroll
        for (int k = 0; k < 2; ++k) {
#pragma unroll
            for (int c = 0; c < 4; ++c) Bf[c][k] = lds_tr(sb + (boff ^ (c << 5)) + k * 4096, 512);
#pragma unroll
            for (int i = 0; i < 4; ++i) Af[i][k] = lds_b128(sb + (aoff ^ (k << 6)) + i * 2048);
        }
        SCHED;
        if (t + 4 < G2_NT) {
            const char* gb = bsrc(t + 4); ST_B(gb, ss, 0); ST_B(gb, ss, 1); ST_A(t + 4, ss, 0); ST_A(t + 4, ss, 1);
            WAIT_V(12);
        } else if (t + 4 == G2_NT) WAIT_V(8); else if (t + 3 == G2_NT) WAIT_V(4); else WAIT_V(0);
        ss = ss == G2_NS - 1 ? 0 : ss + 1;
        WAIT_L(0); BAR;
        const bool more = t + 5 < G2_NT;
        const char* gb = bsrc(more ? t + 5 : t);
        __builtin_amdgcn_s_setprio(1);
#pragma unroll
        for (int k = 0; k < 2; ++k)
#pragma unroll
            for (int c = 0; c < 4; ++c) {
#pragma unroll
                for (int i = 0; i < 4; ++i) acc[i][c] = mfma16(Bf[c][k], Af[i][k], acc[i][c]);
                if (c & 1) {
                    SCHED;
                    if (more) { if (k == 0) ST_B(gb, ss, c >> 1); else ST_A(t + 5, ss, c >> 1); }
                    SCHED;
                }
            }
        __builtin_amdgcn_s_setprio(0);
        if (more) WAIT_V(12);
        else if (t + 5 == G2_NT) WAIT_V(8); else if (t + 4 == G2_NT) WAIT_V(4); else WAIT_V(0);
        ss = ss == G2_NS - 1 ? 0 : ss + 1;
        BAR; SCHED;
        slot += 2; if (slot >= G2_NS) slot -= G2_NS;
    }
    if (G == 0) BAR;
#undef ST_A
#undef ST_B
    STAMP(1, 2);
    f32x4 keep[2][4];
    {
        LAS f32x4* wr = (LAS f32x4*)(lds + wid * 8192) + lane;
        LAS f32x4* rd = (LAS f32x4*)(lds + (wid ^ 4) * 8192) + lane;
#pragma unroll
        for (int u = 0; u < 2; ++u)
#pragma unroll
            for (int c = 0; c < 4; ++c) {
                wr[(u * 4 + c) * 64] = sel4(G, acc[u][c], acc[2 + u][c]);
                keep[u][c] = sel4(G, acc[2 + u][c], acc[u][c]);
            }
        WAIT_L(0); BAR;
#pragma unroll
        for (int u = 0; u < 2; ++u)
#pragma unroll
            for (int c = 0; c < 4; ++c) keep[u][c] += rd[(u * 4 + c) * 64];
    }
    const int pair = (mt * 8 + nt) * 2;
    const __amdgpu_buffer_rsrc_t rs = __builtin_amdgcn_make_buffer_rsrc((void*)xbuf, 0, 256 * 32768, 0x00020000);
    const int xo = wid * 4096 + lane * 16;
#pragma unroll
    for (int c = 0; c < 4; ++c) {
        const f32x4 v = sel4(sp, keep[0][c], keep[1][c]);
        __builtin_amdgcn_raw_buffer_store_b128(__builtin_bit_cast(u32x4, v), rs, (pair + sp) * 32768 + xo + c * 1024, 0, 16);
    }
    u32x2 xr[4];
#pragma unroll
    for (int c = 0; c < 4; ++c) xr[c] = *(const u32x2*)(x + (size_t)frow * D_ + n0 + wn * 64 + fq * 4 + c * 16);
    WAIT_V(0);
    __syncthreads();
    STAMP(1, 3);
    if (wid == 0) {
        if (lane == 0) __hip_atomic_store((GAS unsigned*)(flags + pair + sp), 1u, __ATOMIC_RELAXED, __HIP_MEMORY_SCOPE_AGENT);
        for (int spin = 0; spin < (1 << 22); ++spin) {
            const unsigned f = __hip_atomic_load((GAS unsigned*)(flags + pair + (sp ^ 1)), __ATOMIC_RELAXED, __HIP_MEMORY_SCOPE_AGENT);
            if (__builtin_amdgcn_readfirstlane(f) == 1u) break;
            __builtin_amdgcn_s_sleep(1);
        }
        __builtin_amdgcn_fence(__ATOMIC_ACQUIRE, "wavefront");
    }
    __syncthreads();
    STAMP(1, 4);
    unsigned pk[4][2];
#pragma unroll
    for (int c = 0; c < 4; ++c) {
        const f32x4 r = __builtin_bit_cast(f32x4, __builtin_amdgcn_raw_buffer_load_b128(rs, (pair + (sp ^ 1)) * 32768 + xo + c * 1024, 0, 16));
        const f32x4 v = sel4(sp, keep[1][c], keep[0][c]) + r;
        pk[c][0] = pack_bf16(v[0] + bf_lo(xr[c][0]), v[1] + bf_hi(xr[c][0]));
        pk[c][1] = pack_bf16(v[2] + bf_lo(xr[c][1]), v[3] + bf_hi(xr[c][1]));
    }
    char* gO = (char*)out + (size_t)frow * (D_ * 2) + (n0 + wn * 64 + (fq & 1) * 16 + (fq >> 1) * 8) * 2;
#pragma unroll
    for (int j = 0; j < 2; ++j) {
        auto rx = __builtin_amdgcn_permlane16_swap(pk[2 * j][0], pk[2 * j + 1][0], false, false);
        auto ry = __builtin_amdgcn_permlane16_swap(pk[2 * j][1], pk[2 * j + 1][1], false, false);
        u32x4 o = {rx[0], ry[0], rx[1], ry[1]};
        *(u32x4*)(gO + j * 64) = o;
    }
    STAMP(1, 5);
}

extern "C" void kernel_launch(void* const* d_in, const int* in_sizes, int n_in, void* d_out, int out_size, void* d_ws,
                              size_t ws_size, hipStream_t stream) {
    const __hip_bfloat16* x = (const __hip_bfloat16*)d_in[0];
    const __hip_bfloat16* wqkv = (const __hip_bfloat16*)d_in[1];
    const __hip_bfloat16* wo = (const __hip_bfloat16*)d_in[2];
    const __hip_bfloat16* w1 = (const __hip_bfloat16*)d_in[4];
    const __hip_bfloat16* w2 = (const __hip_bfloat16*)d_in[5];
    char* ws = (char*)d_ws;
    __hip_bfloat16* xn = (__hip_bfloat16*)ws;
    __hip_bfloat16* act = (__hip_bfloat16*)(ws + (4u << 20));
    float* xbuf = (float*)(ws + (24u << 20));
    unsigned* flags = (unsigned*)(ws + (32u << 20));
    (void)hipFuncSetAttribute((const void*)gemm1_kernel, hipFuncAttributeMaxDynamicSharedMemorySize, G1_LDS);
    (void)hipFuncSetAttribute((const void*)gemm2_kernel, hipFuncAttributeMaxDynamicSharedMemorySize, G2_LDS);
    ln_kernel<<<S_ / 4, 256, 0, stream>>>(x, xn, flags);
    gemm1_kernel<<<256, 512, G1_LDS, stream>>>(xn, wqkv, w1, act, wo, w2);
    gemm2_kernel<<<256, 512, G2_LDS, stream>>>(act, wo, w2, x, (__hip_bfloat16*)d_out, xbuf, flags);
}
```

```cpp
#include <hip/hip_runtime.h>
#include <hip/hip_bf16.h>
#include <stdint.h>

#define LAS __attribute__((address_space(3)))
#define GAS __attribute__((address_space(1)))
typedef __attribute__((ext_vector_type(8))) short bf16x8;
typedef __attribute__((ext_vector_type(4))) short s16x4;
typedef __attribute__((ext_vector_type(4))) float f32x4;
typedef __attribute__((ext_vector_type(2))) float f32x2;
typedef __attribute__((ext_vector_type(2))) __bf16 bf16x2_t;
typedef __attribute__((ext_vector_type(4))) unsigned u32x4;
typedef __attribute__((ext_vector_type(2))) unsigned u32x2;

constexpr int S_ = 2048, D_ = 1024, DFF = 3584, KACT = D_ + DFF;

#define WAIT_V(n) asm volatile("s_waitcnt vmcnt(" #n ")" ::: "memory")
#define WAIT_L(n) asm volatile("s_waitcnt lgkmcnt(" #n ")" ::: "memory")
#define BAR do { __builtin_amdgcn_s_barrier(); asm volatile("" ::: "memory"); } while (0)
#define SCHED __builtin_amdgcn_sched_barrier(0)
#define STAMP(k, i)
#define GLDS(src, dst) __builtin_amdgcn_global_load_lds((const unsigned*)(src), (LAS unsigned*)(dst), 16, 0, 0)

__device__ __forceinline__ void glds_s(const void* sbase, unsigned voff, unsigned ldsaddr) {
    asm volatile("s_mov_b32 m0, %2\n\ts_nop 0\n\tglobal_load_lds_dwordx4 %0, %1" ::"v"(voff), "s"(sbase), "s"(ldsaddr) : "memory");
}
__device__ __forceinline__ void glds4_s(const void* sbase, unsigned voff, unsigned ldsaddr) {
    asm volatile("s_mov_b32 m0, %2\n\ts_nop 0\n\tglobal_load_lds_dword %0, %1" ::"v"(voff), "s"(sbase), "s"(ldsaddr) : "memory");
}
__device__ __forceinline__ unsigned pack_bf16(float a, float b) {
    f32x2 v = {a, b};
    bf16x2_t r = __builtin_convertvector(v, bf16x2_t);
    return __builtin_bit_cast(unsigned, r);
}
__device__ __forceinline__ float bf_lo(unsigned u) { return __builtin_bit_cast(float, u << 16); }
__device__ __forceinline__ float bf_hi(unsigned u) { return __builtin_bit_cast(float, u & 0xffff0000u); }
__device__ __forceinline__ float rbf(float a) { return bf_lo(pack_bf16(a, a)); }
__device__ __forceinline__ bf16x8 lds_b128(LAS unsigned char* p) { return *(LAS bf16x8*)p; }
__device__ __forceinline__ bf16x8 lds_tr(LAS unsigned char* p, int hi_off) {
    s16x4 lo = __builtin_amdgcn_ds_read_tr16_b64_v4i16((LAS s16x4*)p);
    s16x4 hi = __builtin_amdgcn_ds_read_tr16_b64_v4i16((LAS s16x4*)(p + hi_off));
    return __builtin_shufflevector(lo, hi, 0, 1, 2, 3, 4, 5, 6, 7);
}
__device__ __forceinline__ f32x4 mfma16(bf16x8 a, bf16x8 b, f32x4 c) {
    return __builtin_amdgcn_mfma_f32_16x16x32_bf16(a, b, c, 0, 0, 0);
}
__device__ __forceinline__ f32x4 sel4(bool c, f32x4 a, f32x4 b) {
    f32x4 r;
#pragma unroll
    for (int e = 0; e < 4; ++e) r[e] = c ? a[e] : b[e];
    return r;
}
__device__ __forceinline__ float dot2bf(unsigned a, unsigned b, float c) {
    return __builtin_amdgcn_fdot2_f32_bf16(__builtin_bit_cast(bf16x2_t, a), __builtin_bit_cast(bf16x2_t, b), c, false);
}

#define LN_PF_BLOCKS 0
__global__ __launch_bounds__(256) void ln_kernel(const __hip_bfloat16* __restrict__ x, __hip_bfloat16* __restrict__ xn,
                                                 unsigned* __restrict__ flags, const char* __restrict__ wqkv, const char* __restrict__ w1) {
    const int lane = threadIdx.x & 63, row = blockIdx.x * 4 + (threadIdx.x >> 6);
    if (blockIdx.x >= S_ / 4) {
        const int pb = blockIdx.x - S_ / 4;
        const char* base = pb < 96 ? wqkv + (size_t)pb * 65536 : w1 + (size_t)(pb - 96) * 65536;
        unsigned t0, t1, t2, t3;
        asm volatile("global_load_dword %0, %4, %5\n\tglobal_load_dword %1, %4, %6\n\tglobal_load_dword %2, %4, %7\n\tglobal_load_dword %3, %4, %8\n\ts_waitcnt vmcnt(0)"
                     : "=&v"(t0), "=&v"(t1), "=&v"(t2), "=&v"(t3)
                     : "v"(threadIdx.x * 64u), "s"(base), "s"(base + 16384), "s"(base + 32768), "s"(base + 49152));
        return;
    }
    if (blockIdx.x == 0) flags[threadIdx.x] = 0u;
    const u32x4* src = (const u32x4*)(x + (size_t)row * D_);
    u32x4 v0 = src[lane], v1 = src[64 + lane];
    float f[16];
#pragma unroll
    for (int i = 0; i < 4; ++i) { f[2 * i] = bf_lo(v0[i]); f[2 * i + 1] = bf_hi(v0[i]); f[8 + 2 * i] = bf_lo(v1[i]); f[9 + 2 * i] = bf_hi(v1[i]); }
    float s = 0.f;
#pragma unroll
    for (int i = 0; i < 16; ++i) s += f[i];
#pragma unroll
    for (int o = 32; o >= 1; o >>= 1) s += __shfl_xor(s, o);
    const float mean = s * (1.0f / D_);
    float q = 0.f;
#pragma unroll
    for (int i = 0; i < 16; ++i) { const float d = f[i] - mean; q += d * d; }
#pragma unroll
    for (int o = 32; o >= 1; o >>= 1) q += __shfl_xor(q, o);
    const float mb = rbf(mean);
    const float sd = rbf(sqrtf(rbf(rbf(q * (1.0f / D_)) + 1e-5f)));
    u32x4 o0, o1;
#pragma unroll
    for (int i = 0; i < 4; ++i) {
        o0[i] = pack_bf16(rbf(f[2 * i] - mb) / sd, rbf(f[2 * i + 1] - mb) / sd);
        o1[i] = pack_bf16(rbf(f[8 + 2 * i] - mb) / sd, rbf(f[9 + 2 * i] - mb) / sd);
    }
    u32x4* dst = (u32x4*)(xn + (size_t)row * D_);
    dst[lane] = o0; dst[64 + lane] = o1;
}

constexpr int G1_ABUF = 34816, G1_BOFF = 3 * G1_ABUF, G1_BBUF = 14336, G1_PFOFF = G1_BOFF + 4 * G1_BBUF, G1_LDS = G1_PFOFF + 2048;
constexpr int ATT_HEAD = 64000, ATT_K = 20480, ATT_V = 20480 + 21760, ATT_RS = 80, H_RS = 464;

template <bool QKV>
__device__ __forceinline__ void gemm1_body(LAS unsigned char* lds, const __hip_bfloat16* __restrict__ xn,
                                           const __hip_bfloat16* __restrict__ w, __hip_bfloat16* __restrict__ act,
                                           const char* __restrict__ pf0, const char* __restrict__ pf1, const int mt, const int ct) {
    constexpr int LDB = QKV ? 3 * D_ : DFF;
    const int tid = threadIdx.x, wid = __builtin_amdgcn_readfirstlane(tid >> 6), lane = tid & 63;
    const int G = wid >> 2, wq = wid & 3, fr = lane & 15, fq = lane >> 4;
    const int m0 = mt * 256;
    int cb0, cstep, cex;
    if (QKV) { cb0 = ct * 64; cstep = D_; cex = 0; }
    else { const int n0 = ct * 224; cb0 = n0 + ((ct & 1) ? 32 : 0); cstep = 64; cex = (ct & 1) ? n0 : n0 + 192; }

    const unsigned lds0 = (unsigned)(uintptr_t)lds;
    const unsigned voffA = (unsigned)((8 * wid + (lane >> 3)) * (D_ * 2) + (((lane & 7) ^ ((4 * wid + (lane >> 4)) & 7)) << 4));
    const unsigned voffB = (unsigned)((8 * wq + (lane >> 3)) * (LDB * 2) + (((lane & 7) ^ ((((lane >> 4) & 1) + 2 * (wq & 1)) << 1)) << 4));
    unsigned voff2 = voffB;
    if (G) {
        if (QKV) voff2 = (unsigned)((4 * wq + ((lane >> 3) & 3)) * (D_ * 2) + (((lane & 7) ^ ((2 * wq + ((lane >> 4) & 1)) & 7)) << 4));
        else voff2 = (unsigned)((8 * wq + ((lane >> 2) & 7)) * (LDB * 2) + ((((lane >> 1) & 1) ^ (wq & 1)) << 5) + ((lane & 1) << 4));
    }
    const char* gA = (const char*)xn + (size_t)m0 * (D_ * 2);
    const char* gB1 = (const char*)w + (size_t)(cb0 + G * cstep) * 2;
    const char* gB2 = G ? (QKV ? (const char*)xn + (size_t)(m0 >= 16 ? m0 - 16 : 0) * (D_ * 2) : (const char*)w + (size_t)cex * 2)
                        : (const char*)w + (size_t)(cb0 + 2 * cstep) * 2;
    constexpr size_t KSB = (size_t)32 * LDB * 2;
    const unsigned dB1 = lds0 + G1_BOFF + G * 4096 + wq * 1024;
    const unsigned dB2 = G ? (QKV ? lds0 + 32768 + wq * 512 : lds0 + G1_BOFF + 12288 + wq * 512) : lds0 + G1_BOFF + 8192 + wq * 1024;
    const bool halo = QKV && G;
    const int lim2 = G ? 32 : 64;
#define STAGE_A(t, kh, ab) do { \
        glds_s(gA + (size_t)(t) * 128 + (size_t)(2 * (kh)) * (64 * D_ * 2), voffA, lds0 + (ab) + (kh) * 16384 + wid * 1024); \
        glds_s(gA + (size_t)(t) * 128 + (size_t)(2 * (kh) + 1) * (64 * D_ * 2), voffA, lds0 + (ab) + (kh) * 16384 + 8192 + wid * 1024); } while (0)
#define STAGE_B(h, s, t, ab) do { \
        glds_s(gB1 + (size_t)(h) * KSB, voffB, dB1 + (s) * G1_BBUF); \
        if (lane < lim2) glds_s(gB2 + (halo ? (size_t)(t) * 128 : (size_t)(h) * KSB), voff2, dB2 + (halo ? (unsigned)(ab) : (unsigned)((s) * G1_BBUF))); } while (0)

    const int q4 = (lane & 15) >> 2, p4 = lane & 3;
    const int aswz = (fq ^ ((fr >> 1) & 7)) << 4;
    const int oA = (wq * 64 + fr) * 128 + aswz, oH = 32768 + fr * 128 + aswz;
    const int bsw = ((q4 >> 1) & 1) + 2 * (fq & 1);
    const int bbase = G1_BOFF + fq * 1024 + q4 * 128 + p4 * 8;
    LAS unsigned char* pB[2] = {lds + bbase + (((2 * G) ^ bsw) << 5), lds + bbase + (((2 * G + 1) ^ bsw) << 5)};
    LAS unsigned char* pEB = QKV ? lds + bbase + (1 + (wq >> 1)) * 4096 + (((2 * G + (wq & 1)) ^ bsw) << 5)
                                 : lds + G1_BOFF + 12288 + fq * 512 + q4 * 64 + ((G ^ (fq & 1)) << 5) + p4 * 8;

    f32x4 acc[4][6], accE[QKV ? 1 : 4];
#pragma unroll
    for (int i = 0; i < 4; ++i)
#pragma unroll
        for (int c = 0; c < 6; ++c) acc[i][c] = (f32x4){0.f, 0.f, 0.f, 0.f};
#pragma unroll
    for (int i = 0; i < (QKV ? 1 : 4); ++i) accE[i] = (f32x4){0.f, 0.f, 0.f, 0.f};
    bf16x8 Af[4], Bf[6], Be, Ae;

#define LOADF(s, kh, ab) do { \
        _Pragma("unroll") for (int _c = 0; _c < 6; ++_c) Bf[_c] = lds_tr(pB[_c & 1] + (s) * G1_BBUF + (_c >> 1) * 4096, 512); \
        Be = lds_tr(pEB + (s) * G1_BBUF, QKV ? 512 : 256); \
        _Pragma("unroll") for (int _i = 0; _i < 4; ++_i) Af[_i] = lds_b128(lds + (ab) + (oA ^ ((kh) << 6)) + _i * 2048); \
        if (QKV) Ae = lds_b128(lds + (ab) + (oH ^ ((kh) << 6))); \
        SCHED; } while (0)
#define MMA() do { WAIT_L(0); BAR; __builtin_amdgcn_s_setprio(1); \
        if (QKV) { accE[0] = mfma16(Be, Ae, accE[0]); } \
        _Pragma("unroll") for (int _c = 0; _c < 6; ++_c) _Pragma("unroll") for (int _i = 0; _i < 4; ++_i) \
            acc[_i][_c] = mfma16(Bf[_c], Af[_i], acc[_i][_c]); \
        if (QKV) { asm volatile("" : "+v"(accE[0])); } \
        else { _Pragma("unroll") for (int _i = 0; _i < 4; ++_i) accE[QKV ? 0 : _i] = mfma16(Be, Af[_i], accE[QKV ? 0 : _i]); } \
        __builtin_amdgcn_s_setprio(0); BAR; SCHED; } while (0)

    STAMP(0, 0);
    STAGE_A(0, 0, 0); STAGE_A(0, 1, 0); STAGE_B(0, 0, 0, 0);
    STAGE_A(1, 0, G1_ABUF); STAGE_B(1, 1, 1, G1_ABUF);
    STAGE_A(1, 1, G1_ABUF); STAGE_B(2, 2, 1, G1_ABUF);
    WAIT_V(8); BAR;
    if (G == 1) BAR;
    STAMP(0, 1);
    int a0 = 0, a1 = G1_ABUF, a2 = 2 * G1_ABUF;
    for (int t = 0; t < 14; t += 2) {
        const int h = 2 * t;
        LOADF(0, 0, a0); STAGE_A(t + 2, 0, a2); STAGE_B(h + 3, 3, t + 2, a2); WAIT_V(8); MMA();
        LOADF(1, 1, a0); STAGE_A(t + 2, 1, a2); STAGE_B(h + 4, 0, t + 2, a2); WAIT_V(8); MMA();
        LOADF(2, 0, a1); STAGE_A(t + 3, 0, a0); STAGE_B(h + 5, 1, t + 3, a0); WAIT_V(8); MMA();
        LOADF(3, 1, a1); STAGE_A(t + 3, 1, a0); STAGE_B(h + 6, 2, t + 3, a0); WAIT_V(8); MMA();
        const int a = a0; a0 = a2; a2 = a1; a1 = a;
    }
    LOADF(0, 0, a0); STAGE_B(31, 3, 15, a1); WAIT_V(6); MMA();
    LOADF(1, 1, a0);
    glds4_s(pf0, tid * 64, lds0 + G1_PFOFF + wid * 256); glds4_s(pf1, lane * 64, lds0 + G1_PFOFF + wid * 256);
    WAIT_V(4); MMA();
    LOADF(2, 0, a1); WAIT_V(2); MMA();
    LOADF(3, 1, a1); MMA();
    STAMP(0, 2);
#undef STAGE_A
#undef STAGE_B
#undef LOADF
#undef MMA

    if (!QKV) {
        const int n0 = ct * 224;
        LAS unsigned char* wp = lds + (wq * 64 + fr) * H_RS + fq * 8;
#pragma unroll
        for (int i = 0; i < 4; ++i) {
#pragma unroll
            for (int c = 0; c < 7; ++c) {
                const f32x4 a = c < 6 ? acc[i][c < 6 ? c : 0] : accE[QKV ? 0 : i];
                const int col = c < 6 ? (cb0 - n0) + (c >> 1) * 64 + (2 * G + (c & 1)) * 16 : (cex - n0) + G * 16;
                u32x2 o2 = {pack_bf16(fmaxf(a[0], 0.f), fmaxf(a[1], 0.f)), pack_bf16(fmaxf(a[2], 0.f), fmaxf(a[3], 0.f))};
                *(LAS u32x2*)(wp + i * 16 * H_RS + col * 2) = o2;
            }
        }
        WAIT_L(0);
        if (G == 0) BAR;
        BAR;
        STAMP(0, 3);
        char* gO = (char*)act + (size_t)m0 * (KACT * 2) + (D_ + n0) * 2;
#pragma unroll
        for (int it = 0; it < 14; ++it) {
            const int idx = it * 512 + tid, row = idx / 28, ch = idx - row * 28;
            const u32x4 v = *(LAS u32x4*)(lds + row * H_RS + ch * 16);
            *(u32x4*)(gO + (size_t)row * (KACT * 2) + ch * 16) = v;
        }
    } else {
        LAS unsigned char* hb = lds + G * ATT_HEAD;
#pragma unroll
        for (int i = 0; i < 4; ++i) {
            const int row = wq * 64 + i * 16 + fr;
#pragma unroll
            for (int c = 0; c < 6; ++c) {
                const f32x4 a = acc[i][c];
                u32x2 o2 = {pack_bf16(a[0], a[1]), pack_bf16(a[2], a[3])};
                const int base = (c >> 1) == 0 ? 0 : ((c >> 1) == 1 ? ATT_K + 16 * ATT_RS : ATT_V + 16 * ATT_RS);
                *(LAS u32x2*)(hb + base + row * ATT_RS + (c & 1) * 32 + fq * 8) = o2;
            }
        }
        {
            const f32x4 a = accE[0];
            u32x2 o2 = {pack_bf16(a[0], a[1]), pack_bf16(a[2], a[3])};
            *(LAS u32x2*)(hb + ((wq >> 1) ? ATT_V : ATT_K) + fr * ATT_RS + (wq & 1) * 32 + fq * 8) = o2;
        }
        WAIT_L(0);
        if (G == 0) BAR;
        BAR;
        STAMP(0, 3);
        const int lrow = wq * 64 + lane, srow = m0 + lrow;
        u32x4 qv[4];
#pragma unroll
        for (int c = 0; c < 4; ++c) qv[c] = *(LAS u32x4*)(hb + lrow * ATT_RS + c * 16);
        float lg[8];
        LAS unsigned char* kp = hb + ATT_K + (lrow + 2) * ATT_RS;
#pragma unroll
        for (int j = 0; j < 8; ++j) {
            float d0 = 0.f, d1 = 0.f;
#pragma unroll
            for (int c = 0; c < 4; ++c) {
                const u32x4 kv = *(LAS u32x4*)(kp + (14 - 2 * j) * ATT_RS + c * 16);
                d0 = dot2bf(qv[c][0], kv[0], d0); d1 = dot2bf(qv[c][1], kv[1], d1);
                d0 = dot2bf(qv[c][2], kv[2], d0); d1 = dot2bf(qv[c][3], kv[3], d1);
            }
            const float l = rbf(rbf(d0 + d1) * 0.17677669529663687f);
            lg[j] = (srow - 2 * j >= 0) ? l : -1e9f;
        }
        float mx = lg[0];
#pragma unroll
        for (int j = 1; j < 8; ++j) mx = fmaxf(mx, lg[j]);
        float sum = 0.f;
#pragma unroll
        for (int j = 0; j < 8; ++j) { lg[j] = __expf(lg[j] - mx); sum += lg[j]; }
        const float inv = 1.0f / sum;
        unsigned pp[4];
#pragma unroll
        for (int j = 0; j < 4; ++j) pp[j] = pack_bf16(lg[2 * j] * inv, lg[2 * j + 1] * inv);
        LAS unsigned char* vp = hb + ATT_V + (lrow + 2) * ATT_RS;
#pragma unroll
        for (int c = 0; c < 4; ++c) {
            float o[8];
#pragma unroll
            for (int e = 0; e < 8; ++e) o[e] = 0.f;
#pragma unroll
            for (int j = 0; j < 4; ++j) {
                const u32x4 va = *(LAS u32x4*)(vp + (14 - 4 * j) * ATT_RS + c * 16);
                const u32x4 vb = *(LAS u32x4*)(vp + (12 - 4 * j) * ATT_RS + c * 16);
#pragma unroll
                for (int e = 0; e < 4; ++e) {
                    o[2 * e] = dot2bf(__builtin_amdgcn_perm(vb[e], va[e], 0x05040100u), pp[j], o[2 * e]);
                    o[2 * e + 1] = dot2bf(__builtin_amdgcn_perm(vb[e], va[e], 0x07060302u), pp[j], o[2 * e + 1]);
                }
            }
            u32x4 ov = {pack_bf16(o[0], o[1]), pack_bf16(o[2], o[3]), pack_bf16(o[4], o[5]), pack_bf16(o[6], o[7])};
            *(LAS u32x4*)(hb + lrow * ATT_RS + c * 16) = ov;
        }
        char* gO = (char*)act + (size_t)(m0 + wq * 64 + (lane >> 2)) * (KACT * 2) + (ct * 64 + G * 32) * 2 + (lane & 3) * 16;
#pragma unroll
        for (int i = 0; i < 4; ++i) {
            const u32x4 v = *(LAS u32x4*)(hb + (wq * 64 + i * 16 + (lane >> 2)) * ATT_RS + (lane & 3) * 16);
            *(u32x4*)(gO + (size_t)(i * 16) * (KACT * 2)) = v;
        }
    }
    STAMP(0, 4);
    STAMP(0, 5);
}

__global__ __launch_bounds__(512, 2) void gemm1_kernel(const __hip_bfloat16* __restrict__ xn, const __hip_bfloat16* __restrict__ wqkv,
                                                       const __hip_bfloat16* __restrict__ w1, __hip_bfloat16* __restrict__ act,
                                                       const __hip_bfloat16* __restrict__ wo, const __hip_bfloat16* __restrict__ w2) {
    extern __shared__ __attribute__((aligned(16))) unsigned char smem[];
    LAS unsigned char* lds = (LAS unsigned char*)smem;
    const int xcd = blockIdx.x & 7, j = blockIdx.x >> 3;
    const int ct = xcd * 2 + (j & 1), mt = j >> 2;
    const int wid = __builtin_amdgcn_readfirstlane(threadIdx.x >> 6);
    const char* pf0 = blockIdx.x < 224 ? (const char*)w2 + (size_t)blockIdx.x * 32768 : (const char*)wo + (size_t)(blockIdx.x - 224) * 32768;
    const char* pf1 = (const char*)wo + (1u << 20) + (size_t)((blockIdx.x * 8 + wid) & 255) * 4096;
    if ((j >> 1) & 1) gemm1_body<true>(lds, xn, wqkv, act, pf0, pf1, mt, ct);
    else gemm1_body<false>(lds, xn, w1, act, pf0, pf1, mt, ct);
}

constexpr int G2_NS = 5, G2_STAGE = 32768, G2_LDS = G2_NS * G2_STAGE, G2_NT = KACT / 64 / 2;

__global__ __launch_bounds__(768, 3) void gemm2_kernel(const __hip_bfloat16* __restrict__ act, const __hip_bfloat16* __restrict__ wo,
                                                       const __hip_bfloat16* __restrict__ w2, const __hip_bfloat16* __restrict__ x,
                                                       __hip_bfloat16* __restrict__ out, unsigned* __restrict__ xbuf, unsigned* __restrict__ flags) {
    extern __shared__ __attribute__((aligned(16))) unsigned char smem[];
    LAS unsigned char* lds = (LAS unsigned char*)smem;
    const int tid = threadIdx.x, wid = __builtin_amdgcn_readfirstlane(tid >> 6), lane = tid & 63;
    const int G = wid >> 2, wq = wid & 3, wm = wq >> 1, wn = wq & 1, fr = lane & 15, fq = lane >> 4;
    const int xcd = blockIdx.x & 7, jb = blockIdx.x >> 3;
    const int sp = xcd & 1, mt = (xcd >> 1) * 4 + (jb & 3), nt = jb >> 2;
    const int m0 = mt * 128, n0 = nt * 128, kt0 = sp * G2_NT;
    const int pair = (mt * 8 + nt) * 2;

    if (G == 2) {
        const unsigned voffA = (unsigned)((8 * wq + (lane >> 3)) * (KACT * 2) + (((lane & 7) ^ ((4 * wq + (lane >> 4)) & 7)) << 4));
        const unsigned voffB = (unsigned)((8 * wq + (lane >> 3)) * (D_ * 2) + (((lane & 7) ^ ((((lane >> 4) & 1) + 2 * (wq & 1)) << 1)) << 4));
        const char* gA = (const char*)act + (size_t)m0 * (KACT * 2) + (size_t)kt0 * 128;
        const unsigned ldsw = (unsigned)(uintptr_t)lds + wq * 1024;
        auto stage = [&](int t, int slot) {
            const int kt = kt0 + t;
            const char* gB = (kt < 16 ? (const char*)wo + (size_t)kt * (64 * D_ * 2) : (const char*)w2 + (size_t)(kt - 16) * (64 * D_ * 2)) + n0 * 2;
            const unsigned d = ldsw + slot * G2_STAGE;
#pragma unroll
            for (int u = 0; u < 4; ++u) glds_s(gB + (size_t)(u & 1) * (32 * D_ * 2) + (u >> 1) * 128, voffB, d + 16384 + (u >> 1) * 8192 + (u & 1) * 4096);
#pragma unroll
            for (int u = 0; u < 4; ++u) glds_s(gA + (size_t)t * 128 + (size_t)u * (32 * KACT * 2), voffA, d + u * 4096);
        };
        for (int t = 0; t < 4; ++t) stage(t, t);
        WAIT_V(24); BAR;
        int ss = 4;
        for (int s = 0; s < G2_NT - 4; ++s) {
            stage(s + 4, ss);
            ss = ss == G2_NS - 1 ? 0 : ss + 1;
            WAIT_V(24); BAR;
        }
        WAIT_V(16); BAR;
        WAIT_V(8); BAR;
        WAIT_V(0); BAR;
        BAR; BAR;
        BAR;
        BAR;
        if (wid == 8) {
            if (lane == 0) __hip_atomic_store((GAS unsigned*)(flags + pair + sp), 1u, __ATOMIC_RELAXED, __HIP_MEMORY_SCOPE_AGENT);
            for (int spin = 0; spin < (1 << 22); ++spin) {
                const unsigned f = __hip_atomic_load((GAS unsigned*)(flags + pair + (sp ^ 1)), __ATOMIC_RELAXED, __HIP_MEMORY_SCOPE_AGENT);
                if (__builtin_amdgcn_readfirstlane(f) == 1u) break;
                __builtin_amdgcn_s_sleep(1);
            }
        }
        BAR;
        return;
    }

    const int q4 = (lane & 15) >> 2, p4 = lane & 3;
    const int aoff = (wm * 64 + fr) * 128 + ((fq ^ ((fr >> 1) & 7)) << 4);
    const int boff = 16384 + wn * 8192 + fq * 1024 + q4 * 128 + ((((q4 >> 1) & 1) + 2 * (fq & 1)) << 5) + p4 * 8;
    const int frow = m0 + wm * 64 + (2 * G + sp) * 16 + fr;
    STAMP(1, 0);
    u32x2 xr[4];
#pragma unroll
    for (int c = 0; c < 4; ++c) xr[c] = *(const u32x2*)(x + (size_t)frow * D_ + n0 + wn * 64 + fq * 4 + c * 16);
    f32x4 acc[4][4];
#pragma unroll
    for (int i = 0; i < 4; ++i)
#pragma unroll
        for (int c = 0; c < 4; ++c) acc[i][c] = (f32x4){0.f, 0.f, 0.f, 0.f};
    bf16x8 Af[4][2], Bf[4][2];

    BAR;
    if (G == 1) BAR;
    STAMP(1, 1);
    int slot = G;
    for (int t = G; t < G2_NT; t += 2) {
        LAS unsigned char* sb = lds + slot * G2_STAGE;
#pragma unroll
        for (int k = 0; k < 2; ++k) {
#pragma unroll
            for (int c = 0; c < 4; ++c) Bf[c][k] = lds_tr(sb + (boff ^ (c << 5)) + k * 4096, 512);
#pragma unroll
            for (int i = 0; i < 4; ++i) Af[i][k] = lds_b128(sb + (aoff ^ (k << 6)) + i * 2048);
        }
        WAIT_L(0); BAR;
        __builtin_amdgcn_s_setprio(1);
#pragma unroll
        for (int k = 0; k < 2; ++k)
#pragma unroll
            for (int c = 0; c < 4; ++c)
#pragma unroll
                for (int i = 0; i < 4; ++i) acc[i][c] = mfma16(Bf[c][k], Af[i][k], acc[i][c]);
        __builtin_amdgcn_s_setprio(0);
        BAR; SCHED;
        slot += 2; if (slot >= G2_NS) slot -= G2_NS;
    }
    STAMP(1, 2);
    f32x4 keep[2][4];
    {
        LAS f32x4* wr = (LAS f32x4*)(lds + wid * 8192) + lane;
        LAS f32x4* rd = (LAS f32x4*)(lds + (wid ^ 4) * 8192) + lane;
#pragma unroll
        for (int u = 0; u < 2; ++u)
#pragma unroll
            for (int c = 0; c < 4; ++c) {
                wr[(u * 4 + c) * 64] = sel4(G, acc[u][c], acc[2 + u][c]);
                keep[u][c] = sel4(G, acc[2 + u][c], acc[u][c]);
            }
        WAIT_L(0);
        if (G == 0) BAR;
        BAR;
#pragma unroll
        for (int u = 0; u < 2; ++u)
#pragma unroll
            for (int c = 0; c < 4; ++c) keep[u][c] += rd[(u * 4 + c) * 64];
    }
    const __amdgpu_buffer_rsrc_t rs = __builtin_amdgcn_make_buffer_rsrc((void*)xbuf, 0, 256 * 16384, 0x00020000);
    const int xo = wid * 2048 + lane * 16;
#pragma unroll
    for (int j = 0; j < 2; ++j) {
        const f32x4 v0 = sel4(sp, keep[0][2 * j], keep[1][2 * j]), v1 = sel4(sp, keep[0][2 * j + 1], keep[1][2 * j + 1]);
        const u32x4 v = {pack_bf16(v0[0], v0[1]), pack_bf16(v0[2], v0[3]), pack_bf16(v1[0], v1[1]), pack_bf16(v1[2], v1[3])};
        __builtin_amdgcn_raw_buffer_store_b128(v, rs, (pair + sp) * 16384 + xo + j * 1024, 0, 16);
    }
    WAIT_V(0);
    BAR;
    STAMP(1, 3);
    BAR;
    STAMP(1, 4);
    unsigned pk[4][2];
#pragma unroll
    for (int j = 0; j < 2; ++j) {
        const u32x4 r = __builtin_amdgcn_raw_buffer_load_b128(rs, (pair + (sp ^ 1)) * 16384 + xo + j * 1024, 0, 16);
#pragma unroll
        for (int e = 0; e < 2; ++e) {
            const int c = 2 * j + e;
            const f32x4 v = sel4(sp, keep[1][c], keep[0][c]);
            pk[c][0] = pack_bf16(v[0] + bf_lo(r[2 * e]) + bf_lo(xr[c][0]), v[1] + bf_hi(r[2 * e]) + bf_hi(xr[c][0]));
            pk[c][1] = pack_bf16(v[2] + bf_lo(r[2 * e + 1]) + bf_lo(xr[c][1]), v[3] + bf_hi(r[2 * e + 1]) + bf_hi(xr[c][1]));
        }
    }
    char* gO = (char*)out + (size_t)frow * (D_ * 2) + (n0 + wn * 64 + (fq & 1) * 16 + (fq >> 1) * 8) * 2;
#pragma unroll
    for (int j = 0; j < 2; ++j) {
        auto rx = __builtin_amdgcn_permlane16_swap(pk[2 * j][0], pk[2 * j + 1][0], false, false);
        auto ry = __builtin_amdgcn_permlane16_swap(pk[2 * j][1], pk[2 * j + 1][1], false, false);
        u32x4 o = {rx[0], ry[0], rx[1], ry[1]};
        *(u32x4*)(gO + j * 64) = o;
    }
    STAMP(1, 5);
}

extern "C" void kernel_launch(void* const* d_in, const int* in_sizes, int n_in, void* d_out, int out_size, void* d_ws,
                              size_t ws_size, hipStream_t stream) {
    const __hip_bfloat16* x = (const __hip_bfloat16*)d_in[0];
    const __hip_bfloat16* wqkv = (const __hip_bfloat16*)d_in[1];
    const __hip_bfloat16* wo = (const __hip_bfloat16*)d_in[2];
    const __hip_bfloat16* w1 = (const __hip_bfloat16*)d_in[4];
    const __hip_bfloat16* w2 = (const __hip_bfloat16*)d_in[5];
    char* ws = (char*)d_ws;
    __hip_bfloat16* xn = (__hip_bfloat16*)ws;
    __hip_bfloat16* act = (__hip_bfloat16*)(ws + (4u << 20));
    unsigned* xbuf = (unsigned*)(ws + (24u << 20));
    unsigned* flags = (unsigned*)(ws + (32u << 20));
    (void)hipFuncSetAttribute((const void*)gemm1_kernel, hipFuncAttributeMaxDynamicSharedMemorySize, G1_LDS);
    (void)hipFuncSetAttribute((const void*)gemm2_kernel, hipFuncAttributeMaxDynamicSharedMemorySize, G2_LDS);
    ln_kernel<<<S_ / 4 + LN_PF_BLOCKS, 256, 0, stream>>>(x, xn, flags, (const char*)wqkv, (const char*)w1);
    gemm1_kernel<<<256, 512, G1_LDS, stream>>>(xn, wqkv, w1, act, wo, w2);
    gemm2_kernel<<<256, 768, G2_LDS, stream>>>(act, wo, w2, x, (__hip_bfloat16*)d_out, xbuf, flags);
}
```

```cpp
#include <hip/hip_runtime.h>
#include <hip/hip_bf16.h>
#include <stdint.h>

#define LAS __attribute__((address_space(3)))
#define GAS __attribute__((address_space(1)))
typedef __attribute__((ext_vector_type(8))) short bf16x8;
typedef __attribute__((ext_vector_type(4))) short s16x4;
typedef __attribute__((ext_vector_type(4))) float f32x4;
typedef __attribute__((ext_vector_type(2))) float f32x2;
typedef __attribute__((ext_vector_type(2))) __bf16 bf16x2_t;
typedef __attribute__((ext_vector_type(4))) unsigned u32x4;
typedef __attribute__((ext_vector_type(2))) unsigned u32x2;

constexpr int S_ = 2048, D_ = 1024, DFF = 3584, KACT = D_ + DFF;
__device__ __forceinline__ size_t act_block(int rt, int kb) { return (size_t)((kb >> 1) * 128 + rt) * 2048 + (size_t)(kb & 1) * 1024; }

#define WAIT_V(n) asm volatile("s_waitcnt vmcnt(" #n ")" ::: "memory")
#define WAIT_L(n) asm volatile("s_waitcnt lgkmcnt(" #n ")" ::: "memory")
#define BAR do { __builtin_amdgcn_s_barrier(); asm volatile("" ::: "memory"); } while (0)
#define SCHED __builtin_amdgcn_sched_barrier(0)
#define STAMP(k, i)
#define GLDS(src, dst) __builtin_amdgcn_global_load_lds((const unsigned*)(src), (LAS unsigned*)(dst), 16, 0, 0)

__device__ __forceinline__ void glds_s(const void* sbase, unsigned voff, unsigned ldsaddr) {
    asm volatile("s_mov_b32 m0, %2\n\ts_nop 0\n\tglobal_load_lds_dwordx4 %0, %1" ::"v"(voff), "s"(sbase), "s"(ldsaddr) : "memory");
}
__device__ __forceinline__ void glds4_s(const void* sbase, unsigned voff, unsigned ldsaddr) {
    asm volatile("s_mov_b32 m0, %2\n\ts_nop 0\n\tglobal_load_lds_dword %0, %1" ::"v"(voff), "s"(sbase), "s"(ldsaddr) : "memory");
}
__device__ __forceinline__ unsigned pack_bf16(float a, float b) {
    f32x2 v = {a, b};
    bf16x2_t r = __builtin_convertvector(v, bf16x2_t);
    return __builtin_bit_cast(unsigned, r);
}
__device__ __forceinline__ float bf_lo(unsigned u) { return __builtin_bit_cast(float, u << 16); }
__device__ __forceinline__ float bf_hi(unsigned u) { return __builtin_bit_cast(float, u & 0xffff0000u); }
__device__ __forceinline__ float rbf(float a) { return bf_lo(pack_bf16(a, a)); }
__device__ __forceinline__ bf16x8 lds_b128(LAS unsigned char* p) { return *(LAS bf16x8*)p; }
__device__ __forceinline__ bf16x8 lds_tr(LAS unsigned char* p, int hi_off) {
    s16x4 lo = __builtin_amdgcn_ds_read_tr16_b64_v4i16((LAS s16x4*)p);
    s16x4 hi = __builtin_amdgcn_ds_read_tr16_b64_v4i16((LAS s16x4*)(p + hi_off));
    return __builtin_shufflevector(lo, hi, 0, 1, 2, 3, 4, 5, 6, 7);
}
__device__ __forceinline__ f32x4 mfma16(bf16x8 a, bf16x8 b, f32x4 c) {
    return __builtin_amdgcn_mfma_f32_16x16x32_bf16(a, b, c, 0, 0, 0);
}
__device__ __forceinline__ f32x4 sel4(bool c, f32x4 a, f32x4 b) {
    f32x4 r;
#pragma unroll
    for (int e = 0; e < 4; ++e) r[e] = c ? a[e] : b[e];
    return r;
}
__device__ __forceinline__ u32x4 pack8(f32x4 a, f32x4 b) {
    return (u32x4){pack_bf16(a[0], a[1]), pack_bf16(a[2], a[3]), pack_bf16(b[0], b[1]), pack_bf16(b[2], b[3])};
}
__device__ __forceinline__ unsigned relu2(unsigned u) {
    typedef __attribute__((ext_vector_type(2))) short s16x2;
    return __builtin_bit_cast(unsigned, __builtin_elementwise_max(__builtin_bit_cast(s16x2, u), (s16x2){0, 0}));
}
__device__ __forceinline__ float u2f(unsigned u) { return __builtin_bit_cast(float, u); }
__device__ __forceinline__ unsigned f2u(float f) { return __builtin_bit_cast(unsigned, f); }
template <bool MAX>
__device__ __forceinline__ float xred4(float v) {
    const auto a = __builtin_amdgcn_permlane16_swap(f2u(v), f2u(v), false, false);
    v = MAX ? fmaxf(u2f(a[0]), u2f(a[1])) : u2f(a[0]) + u2f(a[1]);
    const auto b = __builtin_amdgcn_permlane32_swap(f2u(v), f2u(v), false, false);
    return MAX ? fmaxf(u2f(b[0]), u2f(b[1])) : u2f(b[0]) + u2f(b[1]);
}

#define LN_PF_BLOCKS 0
__global__ __launch_bounds__(256) void ln_kernel(const __hip_bfloat16* __restrict__ x, __hip_bfloat16* __restrict__ xn,
                                                 unsigned* __restrict__ flags, const char* __restrict__ wqkv, const char* __restrict__ w1) {
    const int lane = threadIdx.x & 63, row = blockIdx.x * 4 + (threadIdx.x >> 6);
    if (blockIdx.x >= S_ / 4) {
        const int pb = blockIdx.x - S_ / 4;
        const char* base = pb < 96 ? wqkv + (size_t)pb * 65536 : w1 + (size_t)(pb - 96) * 65536;
        unsigned t0, t1, t2, t3;
        asm volatile("global_load_dword %0, %4, %5\n\tglobal_load_dword %1, %4, %6\n\tglobal_load_dword %2, %4, %7\n\tglobal_load_dword %3, %4, %8\n\ts_waitcnt vmcnt(0)"
                     : "=&v"(t0), "=&v"(t1), "=&v"(t2), "=&v"(t3)
                     : "v"(threadIdx.x * 64u), "s"(base), "s"(base + 16384), "s"(base + 32768), "s"(base + 49152));
        return;
    }
    if (blockIdx.x == 0) flags[threadIdx.x] = 0u;
    const u32x4* src = (const u32x4*)(x + (size_t)row * D_);
    u32x4 v0 = src[lane], v1 = src[64 + lane];
    float f[16];
#pragma unroll
    for (int i = 0; i < 4; ++i) { f[2 * i] = bf_lo(v0[i]); f[2 * i + 1] = bf_hi(v0[i]); f[8 + 2 * i] = bf_lo(v1[i]); f[9 + 2 * i] = bf_hi(v1[i]); }
    float s = 0.f;
#pragma unroll
    for (int i = 0; i < 16; ++i) s += f[i];
#pragma unroll
    for (int o = 32; o >= 1; o >>= 1) s += __shfl_xor(s, o);
    const float mean = s * (1.0f / D_);
    float q = 0.f;
#pragma unroll
    for (int i = 0; i < 16; ++i) { const float d = f[i] - mean; q += d * d; }
#pragma unroll
    for (int o = 32; o >= 1; o >>= 1) q += __shfl_xor(q, o);
    const float mb = rbf(mean);
    const float sd = rbf(sqrtf(rbf(rbf(q * (1.0f / D_)) + 1e-5f)));
    u32x4 o0, o1;
#pragma unroll
    for (int i = 0; i < 4; ++i) {
        o0[i] = pack_bf16(rbf(f[2 * i] - mb) / sd, rbf(f[2 * i + 1] - mb) / sd);
        o1[i] = pack_bf16(rbf(f[8 + 2 * i] - mb) / sd, rbf(f[9 + 2 * i] - mb) / sd);
    }
    u32x4* dst = (u32x4*)(xn + (size_t)row * D_);
    dst[lane] = o0; dst[64 + lane] = o1;
}

constexpr int G1_ABUF = 34816, G1_BOFF = 3 * G1_ABUF, G1_BBUF = 14336, G1_PFOFF = G1_BOFF + 4 * G1_BBUF, G1_LDS = G1_PFOFF + 2048;

template <bool QKV>
__device__ __forceinline__ void gemm1_body(LAS unsigned char* lds, const __hip_bfloat16* __restrict__ xn,
                                           const __hip_bfloat16* __restrict__ w, __hip_bfloat16* __restrict__ act,
                                           const char* __restrict__ pf0, const char* __restrict__ pf1, const int mt, const int ct) {
    constexpr int LDB = QKV ? 3 * D_ : DFF;
    const int tid = threadIdx.x, wid = __builtin_amdgcn_readfirstlane(tid >> 6), lane = tid & 63;
    const int G = wid >> 2, wq = wid & 3, fr = lane & 15, fq = lane >> 4;
    const int m0 = mt * 256;
    int cb0, cstep, cex;
    if (QKV) { cb0 = ct * 64; cstep = D_; cex = 0; }
    else { const int n0 = ct * 224; cb0 = n0 + ((ct & 1) ? 32 : 0); cstep = 64; cex = (ct & 1) ? n0 : n0 + 192; }

    const unsigned lds0 = (unsigned)(uintptr_t)lds;
    const unsigned voffA = (unsigned)((8 * wid + (lane >> 3)) * (D_ * 2) + (((lane & 7) ^ ((4 * wid + (lane >> 4)) & 7)) << 4));
    const unsigned voffB = (unsigned)((8 * wq + (lane >> 3)) * (LDB * 2) + (((lane & 7) ^ ((((lane >> 4) & 1) + 2 * (wq & 1)) << 1)) << 4));
    unsigned voff2 = voffB;
    if (G) {
        if (QKV) voff2 = (unsigned)((4 * wq + ((lane >> 3) & 3)) * (D_ * 2) + (((lane & 7) ^ ((2 * wq + ((lane >> 4) & 1)) & 7)) << 4));
        else voff2 = (unsigned)((8 * wq + ((lane >> 2) & 7)) * (LDB * 2) + ((((lane >> 1) & 1) ^ (wq & 1)) << 5) + ((lane & 1) << 4));
    }
    const char* gA = (const char*)xn + (size_t)m0 * (D_ * 2);
    const char* gB1 = (const char*)w + (size_t)(cb0 + G * cstep) * 2;
    const char* gB2 = G ? (QKV ? (const char*)xn + (size_t)(m0 >= 16 ? m0 - 16 : 0) * (D_ * 2) : (const char*)w + (size_t)cex * 2)
                        : (const char*)w + (size_t)(cb0 + 2 * cstep) * 2;
    constexpr size_t KSB = (size_t)32 * LDB * 2;
    const unsigned dB1 = lds0 + G1_BOFF + G * 4096 + wq * 1024;
    const unsigned dB2 = G ? (QKV ? lds0 + 32768 + wq * 512 : lds0 + G1_BOFF + 12288 + wq * 512) : lds0 + G1_BOFF + 8192 + wq * 1024;
    const bool halo = QKV && G;
    const int lim2 = G ? 32 : 64;
#define STAGE_A(t, kh, ab) do { \
        glds_s(gA + (size_t)(t) * 128 + (size_t)(2 * (kh)) * (64 * D_ * 2), voffA, lds0 + (ab) + (kh) * 16384 + wid * 1024); \
        glds_s(gA + (size_t)(t) * 128 + (size_t)(2 * (kh) + 1) * (64 * D_ * 2), voffA, lds0 + (ab) + (kh) * 16384 + 8192 + wid * 1024); } while (0)
#define STAGE_B(h, s, t, ab) do { \
        glds_s(gB1 + (size_t)(h) * KSB, voffB, dB1 + (s) * G1_BBUF); \
        if (lane < lim2) glds_s(gB2 + (halo ? (size_t)(t) * 128 : (size_t)(h) * KSB), voff2, dB2 + (halo ? (unsigned)(ab) : (unsigned)((s) * G1_BBUF))); } while (0)

    const int q4 = (lane & 15) >> 2, p4 = lane & 3;
    const int aswz = (fq ^ ((fr >> 1) & 7)) << 4;
    const int oA = (wq * 64 + fr) * 128 + aswz, oH = 32768 + fr * 128 + aswz;
    const int bsw = ((q4 >> 1) & 1) + 2 * (fq & 1);
    const int bbase = G1_BOFF + fq * 1024 + q4 * 128 + p4 * 8;
    LAS unsigned char* pB[2] = {lds + bbase + (((2 * G) ^ bsw) << 5), lds + bbase + (((2 * G + 1) ^ bsw) << 5)};
    LAS unsigned char* pEB = lds + G1_BOFF + 12288 + fq * 512 + q4 * 64 + ((G ^ (fq & 1)) << 5) + p4 * 8;

    f32x4 acc[4][6], accE[4];
#pragma unroll
    for (int i = 0; i < 4; ++i) {
        accE[i] = (f32x4){0.f, 0.f, 0.f, 0.f};
#pragma unroll
        for (int c = 0; c < 6; ++c) acc[i][c] = (f32x4){0.f, 0.f, 0.f, 0.f};
    }
    bf16x8 Af[4], Bf[6], Ex;

#define LOADF(s, kh, ab) do { \
        _Pragma("unroll") for (int _c = 0; _c < 6; ++_c) Bf[_c] = lds_tr(pB[_c & 1] + (s) * G1_BBUF + (_c >> 1) * 4096, 512); \
        if (QKV) Ex = lds_b128(lds + (ab) + (oH ^ ((kh) << 6))); else Ex = lds_tr(pEB + (s) * G1_BBUF, 256); \
        _Pragma("unroll") for (int _i = 0; _i < 4; ++_i) Af[_i] = lds_b128(lds + (ab) + (oA ^ ((kh) << 6)) + _i * 2048); \
        SCHED; } while (0)
#define MMA() do { WAIT_L(0); BAR; __builtin_amdgcn_s_setprio(1); \
        _Pragma("unroll") for (int _c = 0; _c < 6; ++_c) _Pragma("unroll") for (int _i = 0; _i < 4; ++_i) \
            acc[_i][_c] = (QKV && _c >= 4) ? mfma16(Af[_i], Bf[_c], acc[_i][_c]) : mfma16(Bf[_c], Af[_i], acc[_i][_c]); \
        if (QKV) { if (wq == 0) { accE[0] = mfma16(Bf[2], Ex, accE[0]); accE[1] = mfma16(Bf[3], Ex, accE[1]); \
                                  accE[2] = mfma16(Ex, Bf[4], accE[2]); accE[3] = mfma16(Ex, Bf[5], accE[3]); } } \
        else { _Pragma("unroll") for (int _i = 0; _i < 4; ++_i) accE[_i] = mfma16(Ex, Af[_i], accE[_i]); } \
        asm volatile("" : "+v"(accE[0]), "+v"(accE[1]), "+v"(accE[2]), "+v"(accE[3])); \
        __builtin_amdgcn_s_setprio(0); BAR; SCHED; } while (0)

    STAMP(0, 0);
    STAGE_A(0, 0, 0); STAGE_A(0, 1, 0); STAGE_B(0, 0, 0, 0);
    STAGE_A(1, 0, G1_ABUF); STAGE_B(1, 1, 1, G1_ABUF);
    STAGE_A(1, 1, G1_ABUF); STAGE_B(2, 2, 1, G1_ABUF);
    WAIT_V(8); BAR;
    if (G == 1) BAR;
    STAMP(0, 1);
    int a0 = 0, a1 = G1_ABUF, a2 = 2 * G1_ABUF;
    for (int t = 0; t < 14; t += 2) {
        const int h = 2 * t;
        LOADF(0, 0, a0); STAGE_A(t + 2, 0, a2); STAGE_B(h + 3, 3, t + 2, a2); WAIT_V(8); MMA();
        LOADF(1, 1, a0); STAGE_A(t + 2, 1, a2); STAGE_B(h + 4, 0, t + 2, a2); WAIT_V(8); MMA();
        LOADF(2, 0, a1); STAGE_A(t + 3, 0, a0); STAGE_B(h + 5, 1, t + 3, a0); WAIT_V(8); MMA();
        LOADF(3, 1, a1); STAGE_A(t + 3, 1, a0); STAGE_B(h + 6, 2, t + 3, a0); WAIT_V(8); MMA();
        const int a = a0; a0 = a2; a2 = a1; a1 = a;
    }
    LOADF(0, 0, a0); STAGE_B(31, 3, 15, a1); WAIT_V(6); MMA();
    LOADF(1, 1, a0);
    glds4_s(pf0, tid * 64, lds0 + G1_PFOFF + wid * 256); glds4_s(pf1, lane * 64, lds0 + G1_PFOFF + wid * 256);
    WAIT_V(4); MMA();
    LOADF(2, 0, a1); WAIT_V(2); MMA();
    LOADF(3, 1, a1); MMA();
    STAMP(0, 2);
#undef STAGE_A
#undef STAGE_B
#undef LOADF
#undef MMA
    const int rt0 = mt * 16 + wq * 4;
    char* gO = (char*)act + lane * 16;
    if (!QKV) {
        if (G == 0) BAR;
#pragma unroll
        for (int i = 0; i < 4; ++i) {
#pragma unroll
            for (int j = 0; j < 3; ++j) {
                const f32x4 a = acc[i][2 * j], b = acc[i][2 * j + 1];
                const u32x4 o = {relu2(pack_bf16(a[0], a[1])), relu2(pack_bf16(a[2], a[3])), relu2(pack_bf16(b[0], b[1])), relu2(pack_bf16(b[2], b[3]))};
                *(u32x4*)(gO + act_block(rt0 + i, ((D_ + cb0) >> 5) + 2 * j + G)) = o;
            }
            const f32x4 e = accE[i];
            const u32x2 o2 = {relu2(pack_bf16(e[0], e[1])), relu2(pack_bf16(e[2], e[3]))};
            *(u32x2*)(gO + act_block(rt0 + i, (D_ + cex) >> 5) + G * 8) = o2;
        }
    } else {
        u32x4 Qf[4], Kf[5], Vf[5];
#pragma unroll
        for (int i = 0; i < 4; ++i) {
            Qf[i] = pack8(acc[i][0], acc[i][1]); Kf[i + 1] = pack8(acc[i][2], acc[i][3]); Vf[i + 1] = pack8(acc[i][4], acc[i][5]);
        }
        LAS u32x4* ex = (LAS u32x4*)(lds + wid * 2048) + lane;
        ex[0] = Kf[4]; ex[64] = Vf[4];
        WAIT_L(0);
        if (G == 0) BAR;
        BAR;
        STAMP(0, 3);
        if (wq == 0) { Kf[0] = pack8(accE[0], accE[1]); Vf[0] = pack8(accE[2], accE[3]); }
        else { Kf[0] = ex[-128]; Vf[0] = ex[-64]; }
        const int par = fr & 1;
        const bool cura = fr - 4 * fq - par >= 0, curb = fr - 4 * fq - par - 2 >= 0;
        const f32x4 z = {0.f, 0.f, 0.f, 0.f};
#pragma unroll
        for (int i = 0; i < 4; ++i) {
            const f32x4 s0 = mfma16(__builtin_bit_cast(bf16x8, Kf[i]), __builtin_bit_cast(bf16x8, Qf[i]), z);
            const f32x4 s1 = mfma16(__builtin_bit_cast(bf16x8, Kf[i + 1]), __builtin_bit_cast(bf16x8, Qf[i]), z);
            float la = cura ? (par ? s1[1] : s1[0]) : (par ? s0[1] : s0[0]);
            float lb = curb ? (par ? s1[3] : s1[2]) : (par ? s0[3] : s0[2]);
            la = rbf(rbf(la) * 0.17677669529663687f); lb = rbf(rbf(lb) * 0.17677669529663687f);
            if (rt0 + i == 0) { la = cura ? la : -1e9f; lb = curb ? lb : -1e9f; }
            const float mx = xred4<true>(fmaxf(la, lb));
            const float ea = __expf(la - mx), eb = __expf(lb - mx);
            const float inv = 1.0f / xred4<false>(ea + eb);
            const unsigned pw = pack_bf16(ea * inv, eb * inv);
            const unsigned wa = (pw & 0xffffu) << (16 * par), wb = (pw >> 16) << (16 * par);
            const u32x4 P = {cura ? 0u : wa, curb ? 0u : wb, cura ? wa : 0u, curb ? wb : 0u};
            const u32x4 V0 = {Vf[i][0], Vf[i][1], Vf[i + 1][0], Vf[i + 1][1]}, V1 = {Vf[i][2], Vf[i][3], Vf[i + 1][2], Vf[i + 1][3]};
            const f32x4 o0 = mfma16(__builtin_bit_cast(bf16x8, V0), __builtin_bit_cast(bf16x8, P), z);
            const f32x4 o1 = mfma16(__builtin_bit_cast(bf16x8, V1), __builtin_bit_cast(bf16x8, P), z);
            *(u32x4*)(gO + act_block(rt0 + i, ct * 2 + G)) = pack8(o0, o1);
        }
    }
    STAMP(0, 4);
    STAMP(0, 5);
}

__global__ __launch_bounds__(512, 2) void gemm1_kernel(const __hip_bfloat16* __restrict__ xn, const __hip_bfloat16* __restrict__ wqkv,
                                                       const __hip_bfloat16* __restrict__ w1, __hip_bfloat16* __restrict__ act,
                                                       const __hip_bfloat16* __restrict__ wo, const __hip_bfloat16* __restrict__ w2) {
    extern __shared__ __attribute__((aligned(16))) unsigned char smem[];
    LAS unsigned char* lds = (LAS unsigned char*)smem;
    const int xcd = blockIdx.x & 7, j = blockIdx.x >> 3;
    const int ct = xcd * 2 + (j & 1), mt = j >> 2;
    const int wid = __builtin_amdgcn_readfirstlane(threadIdx.x >> 6);
    const char* pf0 = blockIdx.x < 224 ? (const char*)w2 + (size_t)blockIdx.x * 32768 : (const char*)wo + (size_t)(blockIdx.x - 224) * 32768;
    const char* pf1 = (const char*)wo + (1u << 20) + (size_t)((blockIdx.x * 8 + wid) & 255) * 4096;
    if ((j >> 1) & 1) gemm1_body<true>(lds, xn, wqkv, act, pf0, pf1, mt, ct);
    else gemm1_body<false>(lds, xn, w1, act, pf0, pf1, mt, ct);
}

constexpr int G2_NS = 5, G2_STAGE = 32768, G2_LDS = G2_NS * G2_STAGE, G2_NT = KACT / 64 / 2;

__global__ __launch_bounds__(768, 3) void gemm2_kernel(const __hip_bfloat16* __restrict__ act, const __hip_bfloat16* __restrict__ wo,
                                                       const __hip_bfloat16* __restrict__ w2, const __hip_bfloat16* __restrict__ x,
                                                       __hip_bfloat16* __restrict__ out, unsigned* __restrict__ xbuf, unsigned* __restrict__ flags) {
    extern __shared__ __attribute__((aligned(16))) unsigned char smem[];
    LAS unsigned char* lds = (LAS unsigned char*)smem;
    const int tid = threadIdx.x, wid = __builtin_amdgcn_readfirstlane(tid >> 6), lane = tid & 63;
    const int G = wid >> 2, wq = wid & 3, wm = wq >> 1, wn = wq & 1, fr = lane & 15, fq = lane >> 4;
    const int xcd = blockIdx.x & 7, jb = blockIdx.x >> 3;
    const int sp = xcd & 1, mt = (xcd >> 1) * 4 + (jb & 3), nt = jb >> 2;
    const int m0 = mt * 128, n0 = nt * 128, kt0 = sp * G2_NT;
    const int pair = (mt * 8 + nt) * 2;

    if (G == 2) {
        const unsigned voffA = (unsigned)(wq * 1024 + lane * 16);
        const unsigned voffB = (unsigned)((8 * wq + (lane >> 3)) * (D_ * 2) + (((lane & 7) ^ ((lane >> 4) << 1)) << 4));
        const char* gA = (const char*)act + act_block(mt * 8, kt0 * 2);
        const unsigned ldsw = (unsigned)(uintptr_t)lds + wq * 1024;
        auto stage = [&](int t, int slot) {
            const int kt = kt0 + t;
            const char* gB = (kt < 16 ? (const char*)wo + (size_t)kt * (64 * D_ * 2) : (const char*)w2 + (size_t)(kt - 16) * (64 * D_ * 2)) + n0 * 2;
            const unsigned d = ldsw + slot * G2_STAGE;
#pragma unroll
            for (int u = 0; u < 4; ++u) glds_s(gB + (size_t)(u & 1) * (32 * D_ * 2) + (u >> 1) * 128, voffB, d + 16384 + (u >> 1) * 8192 + (u & 1) * 4096);
#pragma unroll
            for (int u = 0; u < 4; ++u) glds_s(gA + (size_t)t * (128 * 2048) + u * 4096, voffA, d + u * 4096);
        };
        for (int t = 0; t < 4; ++t) stage(t, t);
        WAIT_V(24); BAR;
        int ss = 4;
        for (int s = 0; s < G2_NT - 4; ++s) {
            stage(s + 4, ss);
            ss = ss == G2_NS - 1 ? 0 : ss + 1;
            WAIT_V(24); BAR;
        }
        WAIT_V(16); BAR;
        WAIT_V(8); BAR;
        WAIT_V(0); BAR;
        BAR; BAR;
        BAR;
        BAR;
        if (wid == 8) {
            if (lane == 0) __hip_atomic_store((GAS unsigned*)(flags + pair + sp), 1u, __ATOMIC_RELAXED, __HIP_MEMORY_SCOPE_AGENT);
            for (int spin = 0; spin < (1 << 22); ++spin) {
                const unsigned f = __hip_atomic_load((GAS unsigned*)(flags + pair + (sp ^ 1)), __ATOMIC_RELAXED, __HIP_MEMORY_SCOPE_AGENT);
                if (__builtin_amdgcn_readfirstlane(f) == 1u) break;
                __builtin_amdgcn_s_sleep(1);
            }
        }
        BAR;
        return;
    }

    const int q4 = (lane & 15) >> 2, p4 = lane & 3;
    const int aoff = wm * 8192 + lane * 16;
    const int boff = 16384 + wn * 8192 + fq * 512 + q4 * 128 + ((((q4 >> 1) & 1) + 2 * (fq & 1)) << 5) + p4 * 8;
    const int frow = m0 + wm * 64 + (2 * G + sp) * 16 + fr;
    STAMP(1, 0);
    f32x4 acc[4][4];
#pragma unroll
    for (int i = 0; i < 4; ++i)
#pragma unroll
        for (int c = 0; c < 4; ++c) acc[i][c] = (f32x4){0.f, 0.f, 0.f, 0.f};
    bf16x8 Af[4][2], Bf[4][2];

    BAR;
    if (G == 1) BAR;
    STAMP(1, 1);
    int slot = G;
    for (int t = G; t < G2_NT; t += 2) {
        LAS unsigned char* sb = lds + slot * G2_STAGE;
#pragma unroll
        for (int k = 0; k < 2; ++k) {
#pragma unroll
            for (int c = 0; c < 4; ++c) Bf[c][k] = lds_tr(sb + (boff ^ (c << 5)) + k * 4096, 2048);
#pragma unroll
            for (int i = 0; i < 4; ++i) Af[i][k] = lds_b128(sb + aoff + i * 2048 + k * 1024);
        }
        WAIT_L(0); BAR;
        __builtin_amdgcn_s_setprio(1);
#pragma unroll
        for (int k = 0; k < 2; ++k)
#pragma unroll
            for (int c = 0; c < 4; ++c)
#pragma unroll
                for (int i = 0; i < 4; ++i) acc[i][c] = mfma16(Bf[c][k], Af[i][k], acc[i][c]);
        __builtin_amdgcn_s_setprio(0);
        BAR; SCHED;
        slot += 2; if (slot >= G2_NS) slot -= G2_NS;
    }
    STAMP(1, 2);
    u32x2 xr[4];
#pragma unroll
    for (int c = 0; c < 4; ++c) xr[c] = *(const u32x2*)(x + (size_t)frow * D_ + n0 + wn * 64 + fq * 4 + c * 16);
    f32x4 keep[2][4];
    {
        LAS f32x4* wr = (LAS f32x4*)(lds + wid * 8192) + lane;
        LAS f32x4* rd = (LAS f32x4*)(lds + (wid ^ 4) * 8192) + lane;
#pragma unroll
        for (int u = 0; u < 2; ++u)
#pragma unroll
            for (int c = 0; c < 4; ++c) {
                wr[(u * 4 + c) * 64] = sel4(G, acc[u][c], acc[2 + u][c]);
                keep[u][c] = sel4(G, acc[2 + u][c], acc[u][c]);
            }
        WAIT_L(0);
        if (G == 0) BAR;
        BAR;
#pragma unroll
        for (int u = 0; u < 2; ++u)
#pragma unroll
            for (int c = 0; c < 4; ++c) keep[u][c] += rd[(u * 4 + c) * 64];
    }
    const __amdgpu_buffer_rsrc_t rs = __builtin_amdgcn_make_buffer_rsrc((void*)xbuf, 0, 256 * 16384, 0x00020000);
    const int xo = wid * 2048 + lane * 16;
#pragma unroll
    for (int j = 0; j < 2; ++j) {
        const f32x4 v0 = sel4(sp, keep[0][2 * j], keep[1][2 * j]), v1 = sel4(sp, keep[0][2 * j + 1], keep[1][2 * j + 1]);
        const u32x4 v = {pack_bf16(v0[0], v0[1]), pack_bf16(v0[2], v0[3]), pack_bf16(v1[0], v1[1]), pack_bf16(v1[2], v1[3])};
        __builtin_amdgcn_raw_buffer_store_b128(v, rs, (pair + sp) * 16384 + xo + j * 1024, 0, 16);
    }
    WAIT_V(0);
    BAR;
    STAMP(1, 3);
    BAR;
    STAMP(1, 4);
    unsigned pk[4][2];
#pragma unroll
    for (int j = 0; j < 2; ++j) {
        const u32x4 r = __builtin_amdgcn_raw_buffer_load_b128(rs, (pair + (sp ^ 1)) * 16384 + xo + j * 1024, 0, 16);
#pragma unroll
        for (int e = 0; e < 2; ++e) {
            const int c = 2 * j + e;
            const f32x4 v = sel4(sp, keep[1][c], keep[0][c]);
            pk[c][0] = pack_bf16(v[0] + bf_lo(r[2 * e]) + bf_lo(xr[c][0]), v[1] + bf_hi(r[2 * e]) + bf_hi(xr[c][0]));
            pk[c][1] = pack_bf16(v[2] + bf_lo(r[2 * e + 1]) + bf_lo(xr[c][1]), v[3] + bf_hi(r[2 * e + 1]) + bf_hi(xr[c][1]));
        }
    }
    char* gO = (char*)out + (size_t)frow * (D_ * 2) + (n0 + wn * 64 + (fq & 1) * 16 + (fq >> 1) * 8) * 2;
#pragma unroll
    for (int j = 0; j < 2; ++j) {
        auto rx = __builtin_amdgcn_permlane16_swap(pk[2 * j][0], pk[2 * j + 1][0], false, false);
        auto ry = __builtin_amdgcn_permlane16_swap(pk[2 * j][1], pk[2 * j + 1][1], false, false);
        u32x4 o = {rx[0], ry[0], rx[1], ry[1]};
        *(u32x4*)(gO + j * 64) = o;
    }
    STAMP(1, 5);
}

extern "C" void kernel_launch(void* const* d_in, const int* in_sizes, int n_in, void* d_out, int out_size, void* d_ws,
                              size_t ws_size, hipStream_t stream) {
    const __hip_bfloat16* x = (const __hip_bfloat16*)d_in[0];
    const __hip_bfloat16* wqkv = (const __hip_bfloat16*)d_in[1];
    const __hip_bfloat16* wo = (const __hip_bfloat16*)d_in[2];
    const __hip_bfloat16* w1 = (const __hip_bfloat16*)d_in[4];
    const __hip_bfloat16* w2 = (const __hip_bfloat16*)d_in[5];
    char* ws = (char*)d_ws;
    __hip_bfloat16* xn = (__hip_bfloat16*)ws;
    __hip_bfloat16* act = (__hip_bfloat16*)(ws + (4u << 20));
    unsigned* xbuf = (unsigned*)(ws + (24u << 20));
    unsigned* flags = (unsigned*)(ws + (32u << 20));
    (void)hipFuncSetAttribute((const void*)gemm1_kernel, hipFuncAttributeMaxDynamicSharedMemorySize, G1_LDS);
    (void)hipFuncSetAttribute((const void*)gemm2_kernel, hipFuncAttributeMaxDynamicSharedMemorySize, G2_LDS);
    ln_kernel<<<S_ / 4 + LN_PF_BLOCKS, 256, 0, stream>>>(x, xn, flags, (const char*)wqkv, (const char*)w1);
    gemm1_kernel<<<256, 512, G1_LDS, stream>>>(xn, wqkv, w1, act, wo, w2);
    gemm2_kernel<<<256, 768, G2_LDS, stream>>>(act, wo, w2, x, (__hip_bfloat16*)d_out, xbuf, flags);
}
```

```cpp
#include <hip/hip_runtime.h>
#include <hip/hip_bf16.h>
#include <stdint.h>

#define LAS __attribute__((address_space(3)))
#define GAS __attribute__((address_space(1)))
typedef __attribute__((ext_vector_type(8))) short bf16x8;
typedef __attribute__((ext_vector_type(4))) short s16x4;
typedef __attribute__((ext_vector_type(4))) float f32x4;
typedef __attribute__((ext_vector_type(2))) float f32x2;
typedef __attribute__((ext_vector_type(2))) __bf16 bf16x2_t;
typedef __attribute__((ext_vector_type(4))) unsigned u32x4;
typedef __attribute__((ext_vector_type(2))) unsigned u32x2;

constexpr int S_ = 2048, D_ = 1024, DFF = 3584, KACT = D_ + DFF;
__device__ __forceinline__ size_t act_block(int rt, int kb) { return (size_t)((kb >> 1) * 128 + rt) * 2048 + (size_t)(kb & 1) * 1024; }

constexpr unsigned XB_EMPTY = 0xffffffffu;

#define WAIT_V(n) asm volatile("s_waitcnt vmcnt(" #n ")" ::: "memory")
#define WAIT_L(n) asm volatile("s_waitcnt lgkmcnt(" #n ")" ::: "memory")
#define BAR do { __builtin_amdgcn_s_barrier(); asm volatile("" ::: "memory"); } while (0)
#define SCHED __builtin_amdgcn_sched_barrier(0)
#define STAMP(k, i)
#define GLDS(src, dst) __builtin_amdgcn_global_load_lds((const unsigned*)(src), (LAS unsigned*)(dst), 16, 0, 0)

__device__ __forceinline__ void glds_s(const void* sbase, unsigned voff, unsigned ldsaddr) {
    asm volatile("s_mov_b32 m0, %2\n\ts_nop 0\n\tglobal_load_lds_dwordx4 %0, %1" ::"v"(voff), "s"(sbase), "s"(ldsaddr) : "memory");
}
__device__ __forceinline__ void glds4_s(const void* sbase, unsigned voff, unsigned ldsaddr) {
    asm volatile("s_mov_b32 m0, %2\n\ts_nop 0\n\tglobal_load_lds_dword %0, %1" ::"v"(voff), "s"(sbase), "s"(ldsaddr) : "memory");
}
__device__ __forceinline__ unsigned pack_bf16(float a, float b) {
    f32x2 v = {a, b};
    bf16x2_t r = __builtin_convertvector(v, bf16x2_t);
    return __builtin_bit_cast(unsigned, r);
}
__device__ __forceinline__ float bf_lo(unsigned u) { return __builtin_bit_cast(float, u << 16); }
__device__ __forceinline__ float bf_hi(unsigned u) { return __builtin_bit_cast(float, u & 0xffff0000u); }
__device__ __forceinline__ float rbf(float a) { return bf_lo(pack_bf16(a, a)); }
__device__ __forceinline__ bf16x8 lds_b128(LAS unsigned char* p) { return *(LAS bf16x8*)p; }
__device__ __forceinline__ bf16x8 lds_tr(LAS unsigned char* p, int hi_off) {
    s16x4 lo = __builtin_amdgcn_ds_read_tr16_b64_v4i16((LAS s16x4*)p);
    s16x4 hi = __builtin_amdgcn_ds_read_tr16_b64_v4i16((LAS s16x4*)(p + hi_off));
    return __builtin_shufflevector(lo, hi, 0, 1, 2, 3, 4, 5, 6, 7);
}
__device__ __forceinline__ f32x4 mfma16(bf16x8 a, bf16x8 b, f32x4 c) {
    return __builtin_amdgcn_mfma_f32_16x16x32_bf16(a, b, c, 0, 0, 0);
}
__device__ __forceinline__ f32x4 sel4(bool c, f32x4 a, f32x4 b) {
    f32x4 r;
#pragma unroll
    for (int e = 0; e < 4; ++e) r[e] = c ? a[e] : b[e];
    return r;
}
__device__ __forceinline__ u32x4 pack8(f32x4 a, f32x4 b) {
    return (u32x4){pack_bf16(a[0], a[1]), pack_bf16(a[2], a[3]), pack_bf16(b[0], b[1]), pack_bf16(b[2], b[3])};
}
__device__ __forceinline__ unsigned relu2(unsigned u) {
    typedef __attribute__((ext_vector_type(2))) short s16x2;
    return __builtin_bit_cast(unsigned, __builtin_elementwise_max(__builtin_bit_cast(s16x2, u), (s16x2){0, 0}));
}
__device__ __forceinline__ float u2f(unsigned u) { return __builtin_bit_cast(float, u); }
__device__ __forceinline__ unsigned f2u(float f) { return __builtin_bit_cast(unsigned, f); }
template <bool MAX>
__device__ __forceinline__ float xred4(float v) {
    const auto a = __builtin_amdgcn_permlane16_swap(f2u(v), f2u(v), false, false);
    v = MAX ? fmaxf(u2f(a[0]), u2f(a[1])) : u2f(a[0]) + u2f(a[1]);
    const auto b = __builtin_amdgcn_permlane32_swap(f2u(v), f2u(v), false, false);
    return MAX ? fmaxf(u2f(b[0]), u2f(b[1])) : u2f(b[0]) + u2f(b[1]);
}

template <int CTRL>
__device__ __forceinline__ float dpp_add(float v) { return v + u2f(__builtin_amdgcn_update_dpp(0u, f2u(v), CTRL, 0xf, 0xf, true)); }
__device__ __forceinline__ float wave_sum(float v) {
    return xred4<false>(dpp_add<0x140>(dpp_add<0x141>(dpp_add<0x4e>(dpp_add<0xb1>(v)))));
}

__global__ __launch_bounds__(256) void ln_kernel(const __hip_bfloat16* __restrict__ x, __hip_bfloat16* __restrict__ xn) {
    const int lane = threadIdx.x & 63, row = blockIdx.x * 4 + (threadIdx.x >> 6);
    const u32x4* src = (const u32x4*)(x + (size_t)row * D_);
    u32x4 v0 = src[lane], v1 = src[64 + lane];
    float f[16];
#pragma unroll
    for (int i = 0; i < 4; ++i) { f[2 * i] = bf_lo(v0[i]); f[2 * i + 1] = bf_hi(v0[i]); f[8 + 2 * i] = bf_lo(v1[i]); f[9 + 2 * i] = bf_hi(v1[i]); }
    float s = 0.f, s1 = 0.f;
#pragma unroll
    for (int i = 0; i < 8; ++i) { s += f[i]; s1 += f[8 + i]; }
    const float mean = wave_sum(s + s1) * (1.0f / D_);
    float q = 0.f, q1 = 0.f;
#pragma unroll
    for (int i = 0; i < 8; ++i) { const float d = f[i] - mean, e = f[8 + i] - mean; q += d * d; q1 += e * e; }
    q = wave_sum(q + q1);
    const float mb = rbf(mean);
    const float ri = 1.0f / rbf(sqrtf(rbf(rbf(q * (1.0f / D_)) + 1e-5f)));
    u32x4 o0, o1;
#pragma unroll
    for (int i = 0; i < 4; ++i) {
        const unsigned d0 = pack_bf16(f[2 * i] - mb, f[2 * i + 1] - mb), d1 = pack_bf16(f[8 + 2 * i] - mb, f[9 + 2 * i] - mb);
        o0[i] = pack_bf16(bf_lo(d0) * ri, bf_hi(d0) * ri);
        o1[i] = pack_bf16(bf_lo(d1) * ri, bf_hi(d1) * ri);
    }
    u32x4* dst = (u32x4*)(xn + (size_t)row * D_);
    dst[lane] = o0; dst[64 + lane] = o1;
}

constexpr int G1_ABUF = 34816, G1_BOFF = 3 * G1_ABUF, G1_BBUF = 14336, G1_PFOFF = G1_BOFF + 4 * G1_BBUF, G1_LDS = G1_PFOFF + 2048;

template <bool QKV>
__device__ __forceinline__ void gemm1_body(LAS unsigned char* lds, const __hip_bfloat16* __restrict__ xn,
                                           const __hip_bfloat16* __restrict__ w, __hip_bfloat16* __restrict__ act,
                                           const char* __restrict__ pf0, const char* __restrict__ pf1, u32x4* __restrict__ xb,
                                           const int mt, const int ct) {
    constexpr int LDB = QKV ? 3 * D_ : DFF;
    const int tid = threadIdx.x, wid = __builtin_amdgcn_readfirstlane(tid >> 6), lane = tid & 63;
    const int G = wid >> 2, wq = wid & 3, fr = lane & 15, fq = lane >> 4;
    const int m0 = mt * 256;
    int cb0, cstep, cex;
    if (QKV) { cb0 = ct * 64; cstep = D_; cex = 0; }
    else { const int n0 = ct * 224; cb0 = n0 + ((ct & 1) ? 32 : 0); cstep = 64; cex = (ct & 1) ? n0 : n0 + 192; }

    const unsigned lds0 = (unsigned)(uintptr_t)lds;
    const unsigned voffA = (unsigned)((8 * wid + (lane >> 3)) * (D_ * 2) + (((lane & 7) ^ ((4 * wid + (lane >> 4)) & 7)) << 4));
    const unsigned voffB = (unsigned)((8 * wq + (lane >> 3)) * (LDB * 2) + (((lane & 7) ^ ((((lane >> 4) & 1) + 2 * (wq & 1)) << 1)) << 4));
    unsigned voff2 = voffB;
    if (G) {
        if (QKV) voff2 = (unsigned)((4 * wq + ((lane >> 3) & 3)) * (D_ * 2) + (((lane & 7) ^ ((2 * wq + ((lane >> 4) & 1)) & 7)) << 4));
        else voff2 = (unsigned)((8 * wq + ((lane >> 2) & 7)) * (LDB * 2) + ((((lane >> 1) & 1) ^ (wq & 1)) << 5) + ((lane & 1) << 4));
    }
    const char* gA = (const char*)xn + (size_t)m0 * (D_ * 2);
    const char* gB1 = (const char*)w + (size_t)(cb0 + G * cstep) * 2;
    const char* gB2 = G ? (QKV ? (const char*)xn + (size_t)(m0 >= 16 ? m0 - 16 : 0) * (D_ * 2) : (const char*)w + (size_t)cex * 2)
                        : (const char*)w + (size_t)(cb0 + 2 * cstep) * 2;
    constexpr size_t KSB = (size_t)32 * LDB * 2;
    const unsigned dB1 = lds0 + G1_BOFF + G * 4096 + wq * 1024;
    const unsigned dB2 = G ? (QKV ? lds0 + 32768 + wq * 512 : lds0 + G1_BOFF + 12288 + wq * 512) : lds0 + G1_BOFF + 8192 + wq * 1024;
    const bool halo = QKV && G;
    const int lim2 = G ? 32 : 64;
#define STAGE_A(t, kh, ab) do { \
        glds_s(gA + (size_t)(t) * 128 + (size_t)(2 * (kh)) * (64 * D_ * 2), voffA, lds0 + (ab) + (kh) * 16384 + wid * 1024); \
        glds_s(gA + (size_t)(t) * 128 + (size_t)(2 * (kh) + 1) * (64 * D_ * 2), voffA, lds0 + (ab) + (kh) * 16384 + 8192 + wid * 1024); } while (0)
#define STAGE_B(h, s, t, ab) do { \
        glds_s(gB1 + (size_t)(h) * KSB, voffB, dB1 + (s) * G1_BBUF); \
        if (lane < lim2) glds_s(gB2 + (halo ? (size_t)(t) * 128 : (size_t)(h) * KSB), voff2, dB2 + (halo ? (unsigned)(ab) : (unsigned)((s) * G1_BBUF))); } while (0)

    const int q4 = (lane & 15) >> 2, p4 = lane & 3;
    const int aswz = (fq ^ ((fr >> 1) & 7)) << 4;
    const int oA = (wq * 64 + fr) * 128 + aswz, oH = 32768 + fr * 128 + aswz;
    const int bsw = ((q4 >> 1) & 1) + 2 * (fq & 1);
    const int bbase = G1_BOFF + fq * 1024 + q4 * 128 + p4 * 8;
    LAS unsigned char* pB[2] = {lds + bbase + (((2 * G) ^ bsw) << 5), lds + bbase + (((2 * G + 1) ^ bsw) << 5)};
    LAS unsigned char* pEB = lds + G1_BOFF + 12288 + fq * 512 + q4 * 64 + ((G ^ (fq & 1)) << 5) + p4 * 8;

    f32x4 acc[4][6], accE[4];
#pragma unroll
    for (int i = 0; i < 4; ++i) {
        accE[i] = (f32x4){0.f, 0.f, 0.f, 0.f};
#pragma unroll
        for (int c = 0; c < 6; ++c) acc[i][c] = (f32x4){0.f, 0.f, 0.f, 0.f};
    }
    bf16x8 Af[4], Bf[6], Ex;

#define LOADF(s, kh, ab) do { \
        _Pragma("unroll") for (int _c = 0; _c < 6; ++_c) Bf[_c] = lds_tr(pB[_c & 1] + (s) * G1_BBUF + (_c >> 1) * 4096, 512); \
        if (QKV) Ex = lds_b128(lds + (ab) + (oH ^ ((kh) << 6))); else Ex = lds_tr(pEB + (s) * G1_BBUF, 256); \
        _Pragma("unroll") for (int _i = 0; _i < 4; ++_i) Af[_i] = lds_b128(lds + (ab) + (oA ^ ((kh) << 6)) + _i * 2048); \
        SCHED; } while (0)
#define MMA() do { WAIT_L(0); BAR; __builtin_amdgcn_s_setprio(1); \
        _Pragma("unroll") for (int _c = 0; _c < 6; ++_c) _Pragma("unroll") for (int _i = 0; _i < 4; ++_i) \
            acc[_i][_c] = (QKV && _c >= 4) ? mfma16(Af[_i], Bf[_c], acc[_i][_c]) : mfma16(Bf[_c], Af[_i], acc[_i][_c]); \
        if (QKV) { if (wq == 0) { accE[0] = mfma16(Bf[2], Ex, accE[0]); accE[1] = mfma16(Bf[3], Ex, accE[1]); \
                                  accE[2] = mfma16(Ex, Bf[4], accE[2]); accE[3] = mfma16(Ex, Bf[5], accE[3]); } } \
        else { _Pragma("unroll") for (int _i = 0; _i < 4; ++_i) accE[_i] = mfma16(Ex, Af[_i], accE[_i]); } \
        asm volatile("" : "+v"(accE[0]), "+v"(accE[1]), "+v"(accE[2]), "+v"(accE[3])); \
        __builtin_amdgcn_s_setprio(0); BAR; SCHED; } while (0)

    STAMP(0, 0);
    STAGE_A(0, 0, 0); STAGE_A(0, 1, 0); STAGE_B(0, 0, 0, 0);
    STAGE_A(1, 0, G1_ABUF); STAGE_B(1, 1, 1, G1_ABUF);
    STAGE_A(1, 1, G1_ABUF); STAGE_B(2, 2, 1, G1_ABUF);
    WAIT_V(8); BAR;
    if (G == 1) BAR;
    STAMP(0, 1);
    int a0 = 0, a1 = G1_ABUF, a2 = 2 * G1_ABUF;
    for (int t = 0; t < 14; t += 2) {
        const int h = 2 * t;
        LOADF(0, 0, a0); STAGE_A(t + 2, 0, a2); STAGE_B(h + 3, 3, t + 2, a2); WAIT_V(8); MMA();
        LOADF(1, 1, a0); STAGE_A(t + 2, 1, a2); STAGE_B(h + 4, 0, t + 2, a2); WAIT_V(8); MMA();
        LOADF(2, 0, a1); STAGE_A(t + 3, 0, a0); STAGE_B(h + 5, 1, t + 3, a0); WAIT_V(8); MMA();
        LOADF(3, 1, a1); STAGE_A(t + 3, 1, a0); STAGE_B(h + 6, 2, t + 3, a0); WAIT_V(8); MMA();
        const int a = a0; a0 = a2; a2 = a1; a1 = a;
    }
    LOADF(0, 0, a0); STAGE_B(31, 3, 15, a1); WAIT_V(6); MMA();
    LOADF(1, 1, a0);
    glds4_s(pf0, tid * 64, lds0 + G1_PFOFF + wid * 256); glds4_s(pf1, lane * 64, lds0 + G1_PFOFF + wid * 256);
    xb[0] = (u32x4){XB_EMPTY, XB_EMPTY, XB_EMPTY, XB_EMPTY}; xb[512] = (u32x4){XB_EMPTY, XB_EMPTY, XB_EMPTY, XB_EMPTY};
    WAIT_V(6); MMA();
    LOADF(2, 0, a1); WAIT_V(4); MMA();
    LOADF(3, 1, a1); MMA();
    STAMP(0, 2);
#undef STAGE_A
#undef STAGE_B
#undef LOADF
#undef MMA
    const int rt0 = mt * 16 + wq * 4;
    char* gO = (char*)act + lane * 16;
    if (!QKV) {
        if (G == 0) BAR;
#pragma unroll
        for (int i = 0; i < 4; ++i) {
#pragma unroll
            for (int j = 0; j < 3; ++j) {
                const f32x4 a = acc[i][2 * j], b = acc[i][2 * j + 1];
                const u32x4 o = {relu2(pack_bf16(a[0], a[1])), relu2(pack_bf16(a[2], a[3])), relu2(pack_bf16(b[0], b[1])), relu2(pack_bf16(b[2], b[3]))};
                *(u32x4*)(gO + act_block(rt0 + i, ((D_ + cb0) >> 5) + 2 * j + G)) = o;
            }
            const f32x4 e = accE[i];
            const u32x2 o2 = {relu2(pack_bf16(e[0], e[1])), relu2(pack_bf16(e[2], e[3]))};
            *(u32x2*)(gO + act_block(rt0 + i, (D_ + cex) >> 5) + G * 8) = o2;
        }
    } else {
        u32x4 Qf[4], Kf[5], Vf[5];
#pragma unroll
        for (int i = 0; i < 4; ++i) {
            Qf[i] = pack8(acc[i][0], acc[i][1]); Kf[i + 1] = pack8(acc[i][2], acc[i][3]); Vf[i + 1] = pack8(acc[i][4], acc[i][5]);
        }
        LAS u32x4* ex = (LAS u32x4*)(lds + wid * 2048) + lane;
        ex[0] = Kf[4]; ex[64] = Vf[4];
        WAIT_L(0);
        if (G == 0) BAR;
        BAR;
        STAMP(0, 3);
        if (wq == 0) { Kf[0] = pack8(accE[0], accE[1]); Vf[0] = pack8(accE[2], accE[3]); }
        else { Kf[0] = ex[-128]; Vf[0] = ex[-64]; }
        const int par = fr & 1;
        const bool cura = fr - 4 * fq - par >= 0, curb = fr - 4 * fq - par - 2 >= 0;
        const f32x4 z = {0.f, 0.f, 0.f, 0.f};
#pragma unroll
        for (int i = 0; i < 4; ++i) {
            const f32x4 s0 = mfma16(__builtin_bit_cast(bf16x8, Kf[i]), __builtin_bit_cast(bf16x8, Qf[i]), z);
            const f32x4 s1 = mfma16(__builtin_bit_cast(bf16x8, Kf[i + 1]), __builtin_bit_cast(bf16x8, Qf[i]), z);
            float la = cura ? (par ? s1[1] : s1[0]) : (par ? s0[1] : s0[0]);
            float lb = curb ? (par ? s1[3] : s1[2]) : (par ? s0[3] : s0[2]);
            la = rbf(rbf(la) * 0.17677669529663687f); lb = rbf(rbf(lb) * 0.17677669529663687f);
            if (rt0 + i == 0) { la = cura ? la : -1e9f; lb = curb ? lb : -1e9f; }
            const float mx = xred4<true>(fmaxf(la, lb));
            const float ea = __expf(la - mx), eb = __expf(lb - mx);
            const float inv = 1.0f / xred4<false>(ea + eb);
            const unsigned pw = pack_bf16(ea * inv, eb * inv);
            const unsigned wa = (pw & 0xffffu) << (16 * par), wb = (pw >> 16) << (16 * par);
            const u32x4 P = {cura ? 0u : wa, curb ? 0u : wb, cura ? wa : 0u, curb ? wb : 0u};
            const u32x4 V0 = {Vf[i][0], Vf[i][1], Vf[i + 1][0], Vf[i + 1][1]}, V1 = {Vf[i][2], Vf[i][3], Vf[i + 1][2], Vf[i + 1][3]};
            const f32x4 o0 = mfma16(__builtin_bit_cast(bf16x8, V0), __builtin_bit_cast(bf16x8, P), z);
            const f32x4 o1 = mfma16(__builtin_bit_cast(bf16x8, V1), __builtin_bit_cast(bf16x8, P), z);
            *(u32x4*)(gO + act_block(rt0 + i, ct * 2 + G)) = pack8(o0, o1);
        }
    }
    STAMP(0, 4);
    STAMP(0, 5);
}

__global__ __launch_bounds__(512, 2) void gemm1_kernel(const __hip_bfloat16* __restrict__ xn, const __hip_bfloat16* __restrict__ wqkv,
                                                       const __hip_bfloat16* __restrict__ w1, __hip_bfloat16* __restrict__ act,
                                                       const __hip_bfloat16* __restrict__ wo, const __hip_bfloat16* __restrict__ w2,
                                                       unsigned* __restrict__ xbuf) {
    extern __shared__ __attribute__((aligned(16))) unsigned char smem[];
    LAS unsigned char* lds = (LAS unsigned char*)smem;
    const int xcd = blockIdx.x & 7, j = blockIdx.x >> 3;
    const int ct = xcd * 2 + (j & 1), mt = j >> 2;
    const int wid = __builtin_amdgcn_readfirstlane(threadIdx.x >> 6);
    const char* pf0 = blockIdx.x < 224 ? (const char*)w2 + (size_t)blockIdx.x * 32768 : (const char*)wo + (size_t)(blockIdx.x - 224) * 32768;
    const char* pf1 = (const char*)wo + (1u << 20) + (size_t)((blockIdx.x * 8 + wid) & 255) * 4096;
    u32x4* xb = (u32x4*)xbuf + blockIdx.x * 1024 + threadIdx.x;
    if ((j >> 1) & 1) gemm1_body<true>(lds, xn, wqkv, act, pf0, pf1, xb, mt, ct);
    else gemm1_body<false>(lds, xn, w1, act, pf0, pf1, xb, mt, ct);
}

constexpr int G2_NS = 5, G2_STAGE = 32768, G2_LDS = G2_NS * G2_STAGE, G2_NT = KACT / 64 / 2;

__global__ __launch_bounds__(768, 3) void gemm2_kernel(const __hip_bfloat16* __restrict__ act, const __hip_bfloat16* __restrict__ wo,
                                                       const __hip_bfloat16* __restrict__ w2, const __hip_bfloat16* __restrict__ x,
                                                       __hip_bfloat16* __restrict__ out, unsigned* __restrict__ xbuf) {
    extern __shared__ __attribute__((aligned(16))) unsigned char smem[];
    LAS unsigned char* lds = (LAS unsigned char*)smem;
    const int tid = threadIdx.x, wid = __builtin_amdgcn_readfirstlane(tid >> 6), lane = tid & 63;
    const int G = wid >> 2, wq = wid & 3, wm = wq >> 1, wn = wq & 1, fr = lane & 15, fq = lane >> 4;
    const int xcd = blockIdx.x & 7, jb = blockIdx.x >> 3;
    const int sp = xcd & 1, mt = (xcd >> 1) * 4 + (jb & 3), nt = jb >> 2;
    const int m0 = mt * 128, n0 = nt * 128, kt0 = sp * G2_NT;
    const int pair = (mt * 8 + nt) * 2;

    if (G == 2) {
        const unsigned voffA = (unsigned)(wq * 1024 + lane * 16);
        const unsigned voffB = (unsigned)((8 * wq + (lane >> 3)) * (D_ * 2) + (((lane & 7) ^ ((lane >> 4) << 1)) << 4));
        const char* gA = (const char*)act + act_block(mt * 8, kt0 * 2);
        const unsigned ldsw = (unsigned)(uintptr_t)lds + wq * 1024;
        auto stage = [&](int t, int slot) {
            const int kt = kt0 + t;
            const char* gB = (kt < 16 ? (const char*)wo + (size_t)kt * (64 * D_ * 2) : (const char*)w2 + (size_t)(kt - 16) * (64 * D_ * 2)) + n0 * 2;
            const unsigned d = ldsw + slot * G2_STAGE;
#pragma unroll
            for (int u = 0; u < 4; ++u) glds_s(gB + (size_t)(u & 1) * (32 * D_ * 2) + (u >> 1) * 128, voffB, d + 16384 + (u >> 1) * 8192 + (u & 1) * 4096);
#pragma unroll
            for (int u = 0; u < 4; ++u) glds_s(gA + (size_t)t * (128 * 2048) + u * 4096, voffA, d + u * 4096);
        };
        for (int t = 0; t < 4; ++t) stage(t, t);
        WAIT_V(24); BAR;
        int ss = 4;
        for (int s = 0; s < G2_NT - 4; ++s) {
            stage(s + 4, ss);
            ss = ss == G2_NS - 1 ? 0 : ss + 1;
            WAIT_V(24); BAR;
        }
        WAIT_V(16); BAR;
        WAIT_V(8); BAR;
        WAIT_V(0); BAR;
        BAR; BAR;
        BAR;
        return;
    }

    const int q4 = (lane & 15) >> 2, p4 = lane & 3;
    const int aoff = wm * 8192 + lane * 16;
    const int boff = 16384 + wn * 8192 + fq * 512 + q4 * 128 + ((((q4 >> 1) & 1) + 2 * (fq & 1)) << 5) + p4 * 8;
    const int frow = m0 + wm * 64 + (2 * G + sp) * 16 + fr;
    STAMP(1, 0);
    f32x4 acc[4][4];
#pragma unroll
    for (int i = 0; i < 4; ++i)
#pragma unroll
        for (int c = 0; c < 4; ++c) acc[i][c] = (f32x4){0.f, 0.f, 0.f, 0.f};
    bf16x8 Af[4][2], Bf[4][2];

    BAR;
    if (G == 1) BAR;
    STAMP(1, 1);
    int slot = G;
    for (int t = G; t < G2_NT; t += 2) {
        LAS unsigned char* sb = lds + slot * G2_STAGE;
#pragma unroll
        for (int k = 0; k < 2; ++k) {
#pragma unroll
            for (int c = 0; c < 4; ++c) Bf[c][k] = lds_tr(sb + (boff ^ (c << 5)) + k * 4096, 2048);
#pragma unroll
            for (int i = 0; i < 4; ++i) Af[i][k] = lds_b128(sb + aoff + i * 2048 + k * 1024);
        }
        WAIT_L(0); BAR;
        __builtin_amdgcn_s_setprio(1);
#pragma unroll
        for (int k = 0; k < 2; ++k)
#pragma unroll
            for (int c = 0; c < 4; ++c)
#pragma unroll
                for (int i = 0; i < 4; ++i) acc[i][c] = mfma16(Bf[c][k], Af[i][k], acc[i][c]);
        __builtin_amdgcn_s_setprio(0);
        BAR; SCHED;
        slot += 2; if (slot >= G2_NS) slot -= G2_NS;
    }
    STAMP(1, 2);
    u32x2 xr[4];
#pragma unroll
    for (int c = 0; c < 4; ++c) xr[c] = *(const u32x2*)(x + (size_t)frow * D_ + n0 + wn * 64 + fq * 4 + c * 16);
    f32x4 keep[2][4];
    {
        LAS f32x4* wr = (LAS f32x4*)(lds + wid * 8192) + lane;
        LAS f32x4* rd = (LAS f32x4*)(lds + (wid ^ 4) * 8192) + lane;
#pragma unroll
        for (int u = 0; u < 2; ++u)
#pragma unroll
            for (int c = 0; c < 4; ++c) {
                wr[(u * 4 + c) * 64] = sel4(G, acc[u][c], acc[2 + u][c]);
                keep[u][c] = sel4(G, acc[2 + u][c], acc[u][c]);
            }
        WAIT_L(0);
        if (G == 0) BAR;
        BAR;
#pragma unroll
        for (int u = 0; u < 2; ++u)
#pragma unroll
            for (int c = 0; c < 4; ++c) keep[u][c] += rd[(u * 4 + c) * 64];
    }
    const __amdgpu_buffer_rsrc_t rs = __builtin_amdgcn_make_buffer_rsrc((void*)xbuf, 0, 256 * 16384, 0x00020000);
    const int xo = wid * 2048 + lane * 16;
#pragma unroll
    for (int j = 0; j < 2; ++j) {
        const f32x4 v0 = sel4(sp, keep[0][2 * j], keep[1][2 * j]), v1 = sel4(sp, keep[0][2 * j + 1], keep[1][2 * j + 1]);
        const u32x4 v = {pack_bf16(v0[0], v0[1]), pack_bf16(v0[2], v0[3]), pack_bf16(v1[0], v1[1]), pack_bf16(v1[2], v1[3])};
        __builtin_amdgcn_raw_buffer_store_b128(v, rs, (pair + sp) * 16384 + xo + j * 1024, 0, 16);
    }
    STAMP(1, 3);
    u32x4 rr[2];
    for (int spin = 0; spin < (1 << 16); ++spin) {
        asm volatile("global_load_dwordx4 %0, %2, %3 sc1\n\tglobal_load_dwordx4 %1, %2, %3 offset:1024 sc1\n\ts_waitcnt vmcnt(0)"
                     : "=&v"(rr[0]), "=&v"(rr[1]) : "v"(xo), "s"((const char*)xbuf + (size_t)(pair + (sp ^ 1)) * 16384) : "memory");
        bool empty = false;
#pragma unroll
        for (int e = 0; e < 4; ++e) empty |= (rr[0][e] == XB_EMPTY) | (rr[1][e] == XB_EMPTY);
        if (__builtin_amdgcn_ballot_w64(empty) == 0) break;
        __builtin_amdgcn_s_sleep(2);
    }
    STAMP(1, 4);
    unsigned pk[4][2];
#pragma unroll
    for (int j = 0; j < 2; ++j) {
        const u32x4 r = rr[j];
#pragma unroll
        for (int e = 0; e < 2; ++e) {
            const int c = 2 * j + e;
            const f32x4 v = sel4(sp, keep[1][c], keep[0][c]);
            pk[c][0] = pack_bf16(v[0] + bf_lo(r[2 * e]) + bf_lo(xr[c][0]), v[1] + bf_hi(r[2 * e]) + bf_hi(xr[c][0]));
            pk[c][1] = pack_bf16(v[2] + bf_lo(r[2 * e + 1]) + bf_lo(xr[c][1]), v[3] + bf_hi(r[2 * e + 1]) + bf_hi(xr[c][1]));
        }
    }
    char* gO = (char*)out + (size_t)frow * (D_ * 2) + (n0 + wn * 64 + (fq & 1) * 16 + (fq >> 1) * 8) * 2;
#pragma unroll
    for (int j = 0; j < 2; ++j) {
        auto rx = __builtin_amdgcn_permlane16_swap(pk[2 * j][0], pk[2 * j + 1][0], false, false);
        auto ry = __builtin_amdgcn_permlane16_swap(pk[2 * j][1], pk[2 * j + 1][1], false, false);
        u32x4 o = {rx[0], ry[0], rx[1], ry[1]};
        *(u32x4*)(gO + j * 64) = o;
    }
    STAMP(1, 5);
}

extern "C" void kernel_launch(void* const* d_in, const int* in_sizes, int n_in, void* d_out, int out_size, void* d_ws,
                              size_t ws_size, hipStream_t stream) {
    const __hip_bfloat16* x = (const __hip_bfloat16*)d_in[0];
    const __hip_bfloat16* wqkv = (const __hip_bfloat16*)d_in[1];
    const __hip_bfloat16* wo = (const __hip_bfloat16*)d_in[2];
    const __hip_bfloat16* w1 = (const __hip_bfloat16*)d_in[4];
    const __hip_bfloat16* w2 = (const __hip_bfloat16*)d_in[5];
    char* ws = (char*)d_ws;
    __hip_bfloat16* xn = (__hip_bfloat16*)ws;
    __hip_bfloat16* act = (__hip_bfloat16*)(ws + (4u << 20));
    unsigned* xbuf = (unsigned*)(ws + (24u << 20));
    (void)hipFuncSetAttribute((const void*)gemm1_kernel, hipFuncAttributeMaxDynamicSharedMemorySize, G1_LDS);
    (void)hipFuncSetAttribute((const void*)gemm2_kernel, hipFuncAttributeMaxDynamicSharedMemorySize, G2_LDS);
    ln_kernel<<<S_ / 4, 256, 0, stream>>>(x, xn);
    gemm1_kernel<<<256, 512, G1_LDS, stream>>>(xn, wqkv, w1, act, wo, w2, xbuf);
    gemm2_kernel<<<256, 768, G2_LDS, stream>>>(act, wo, w2, x, (__hip_bfloat16*)d_out, xbuf);
}
```

```cpp
#include <hip/hip_runtime.h>
#include <hip/hip_bf16.h>
#include <stdint.h>

#define LAS __attribute__((address_space(3)))
#define GAS __attribute__((address_space(1)))
typedef __attribute__((ext_vector_type(8))) short bf16x8;
typedef __attribute__((ext_vector_type(4))) short s16x4;
typedef __attribute__((ext_vector_type(4))) float f32x4;
typedef __attribute__((ext_vector_type(2))) float f32x2;
typedef __attribute__((ext_vector_type(2))) __bf16 bf16x2_t;
typedef __attribute__((ext_vector_type(4))) unsigned u32x4;
typedef __attribute__((ext_vector_type(2))) unsigned u32x2;

constexpr int S_ = 2048, D_ = 1024, DFF = 3584, KACT = D_ + DFF;
__device__ __forceinline__ size_t act_block(int rt, int kb) { return (size_t)((kb >> 1) * 128 + rt) * 2048 + (size_t)(kb & 1) * 1024; }

constexpr unsigned XB_EMPTY = 0xffffffffu;

#define WAIT_V(n) asm volatile("s_waitcnt vmcnt(" #n ")" ::: "memory")
#define WAIT_L(n) asm volatile("s_waitcnt lgkmcnt(" #n ")" ::: "memory")
#define BAR do { __builtin_amdgcn_s_barrier(); asm volatile("" ::: "memory"); } while (0)
#define SCHED __builtin_amdgcn_sched_barrier(0)
#define STAMP(k, i)
#define GLDS(src, dst) __builtin_amdgcn_global_load_lds((const unsigned*)(src), (LAS unsigned*)(dst), 16, 0, 0)

__device__ __forceinline__ void glds_s(const void* sbase, unsigned voff, unsigned ldsaddr) {
    asm volatile("s_mov_b32 m0, %2\n\ts_nop 0\n\tglobal_load_lds_dwordx4 %0, %1" ::"v"(voff), "s"(sbase), "s"(ldsaddr) : "memory");
}
__device__ __forceinline__ void glds4_s(const void* sbase, unsigned voff, unsigned ldsaddr) {
    asm volatile("s_mov_b32 m0, %2\n\ts_nop 0\n\tglobal_load_lds_dword %0, %1" ::"v"(voff), "s"(sbase), "s"(ldsaddr) : "memory");
}
__device__ __forceinline__ unsigned pack_bf16(float a, float b) {
    f32x2 v = {a, b};
    bf16x2_t r = __builtin_convertvector(v, bf16x2_t);
    return __builtin_bit_cast(unsigned, r);
}
__device__ __forceinline__ float bf_lo(unsigned u) { return __builtin_bit_cast(float, u << 16); }
__device__ __forceinline__ float bf_hi(unsigned u) { return __builtin_bit_cast(float, u & 0xffff0000u); }
__device__ __forceinline__ float rbf(float a) { return bf_lo(pack_bf16(a, a)); }
__device__ __forceinline__ bf16x8 lds_b128(LAS unsigned char* p) { return *(LAS bf16x8*)p; }
__device__ __forceinline__ bf16x8 lds_tr(LAS unsigned char* p, int hi_off) {
    s16x4 lo = __builtin_amdgcn_ds_read_tr16_b64_v4i16((LAS s16x4*)p);
    s16x4 hi = __builtin_amdgcn_ds_read_tr16_b64_v4i16((LAS s16x4*)(p + hi_off));
    return __builtin_shufflevector(lo, hi, 0, 1, 2, 3, 4, 5, 6, 7);
}
__device__ __forceinline__ f32x4 mfma16(bf16x8 a, bf16x8 b, f32x4 c) {
    return __builtin_amdgcn_mfma_f32_16x16x32_bf16(a, b, c, 0, 0, 0);
}
__device__ __forceinline__ f32x4 sel4(bool c, f32x4 a, f32x4 b) {
    f32x4 r;
#pragma unroll
    for (int e = 0; e < 4; ++e) r[e] = c ? a[e] : b[e];
    return r;
}
__device__ __forceinline__ u32x4 pack8(f32x4 a, f32x4 b) {
    return (u32x4){pack_bf16(a[0], a[1]), pack_bf16(a[2], a[3]), pack_bf16(b[0], b[1]), pack_bf16(b[2], b[3])};
}
__device__ __forceinline__ unsigned relu2(unsigned u) {
    typedef __attribute__((ext_vector_type(2))) short s16x2;
    return __builtin_bit_cast(unsigned, __builtin_elementwise_max(__builtin_bit_cast(s16x2, u), (s16x2){0, 0}));
}
__device__ __forceinline__ float u2f(unsigned u) { return __builtin_bit_cast(float, u); }
__device__ __forceinline__ unsigned f2u(float f) { return __builtin_bit_cast(unsigned, f); }
template <bool MAX>
__device__ __forceinline__ float xred4(float v) {
    const auto a = __builtin_amdgcn_permlane16_swap(f2u(v), f2u(v), false, false);
    v = MAX ? fmaxf(u2f(a[0]), u2f(a[1])) : u2f(a[0]) + u2f(a[1]);
    const auto b = __builtin_amdgcn_permlane32_swap(f2u(v), f2u(v), false, false);
    return MAX ? fmaxf(u2f(b[0]), u2f(b[1])) : u2f(b[0]) + u2f(b[1]);
}

template <int CTRL>
__device__ __forceinline__ float dpp_add(float v) { return v + u2f(__builtin_amdgcn_update_dpp(0u, f2u(v), CTRL, 0xf, 0xf, true)); }
__device__ __forceinline__ float wave_sum(float v) {
    return xred4<false>(dpp_add<0x140>(dpp_add<0x141>(dpp_add<0x4e>(dpp_add<0xb1>(v)))));
}

__global__ __launch_bounds__(256) void ln_kernel(const __hip_bfloat16* __restrict__ x, __hip_bfloat16* __restrict__ xn) {
    const int lane = threadIdx.x & 63, row = blockIdx.x * 4 + (threadIdx.x >> 6);
    const u32x4* src = (const u32x4*)(x + (size_t)row * D_);
    u32x4 v0 = src[lane], v1 = src[64 + lane];
    float f[16];
#pragma unroll
    for (int i = 0; i < 4; ++i) { f[2 * i] = bf_lo(v0[i]); f[2 * i + 1] = bf_hi(v0[i]); f[8 + 2 * i] = bf_lo(v1[i]); f[9 + 2 * i] = bf_hi(v1[i]); }
    float s = 0.f, s1 = 0.f;
#pragma unroll
    for (int i = 0; i < 8; ++i) { s += f[i]; s1 += f[8 + i]; }
    const float mean = wave_sum(s + s1) * (1.0f / D_);
    float q = 0.f, q1 = 0.f;
#pragma unroll
    for (int i = 0; i < 8; ++i) { const float d = f[i] - mean, e = f[8 + i] - mean; q += d * d; q1 += e * e; }
    q = wave_sum(q + q1);
    const float mb = rbf(mean);
    const float ri = 1.0f / rbf(sqrtf(rbf(rbf(q * (1.0f / D_)) + 1e-5f)));
    u32x4 o0, o1;
#pragma unroll
    for (int i = 0; i < 4; ++i) {
        const unsigned d0 = pack_bf16(f[2 * i] - mb, f[2 * i + 1] - mb), d1 = pack_bf16(f[8 + 2 * i] - mb, f[9 + 2 * i] - mb);
        o0[i] = pack_bf16(bf_lo(d0) * ri, bf_hi(d0) * ri);
        o1[i] = pack_bf16(bf_lo(d1) * ri, bf_hi(d1) * ri);
    }
    u32x4* dst = (u32x4*)(xn + (size_t)row * D_);
    dst[lane] = o0; dst[64 + lane] = o1;
}

constexpr int G1_ABUF = 34816, G1_BOFF = 3 * G1_ABUF, G1_BBUF = 14336, G1_PFOFF = G1_BOFF + 4 * G1_BBUF, G1_LDS = G1_PFOFF + 2048;

template <bool QKV>
__device__ __forceinline__ void gemm1_body(LAS unsigned char* lds, const __hip_bfloat16* __restrict__ xn,
                                           const __hip_bfloat16* __restrict__ w, __hip_bfloat16* __restrict__ act,
                                           const char* __restrict__ pf0, const char* __restrict__ pf1, u32x4* __restrict__ xb,
                                           const int mt, const int ct) {
    constexpr int LDB = QKV ? 3 * D_ : DFF;
    const int tid = threadIdx.x, wid = __builtin_amdgcn_readfirstlane(tid >> 6), lane = tid & 63;
    const int G = wid >> 2, wq = wid & 3, fr = lane & 15, fq = lane >> 4;
    const int m0 = mt * 256;
    int cb0, cstep, cex;
    if (QKV) { cb0 = ct * 64; cstep = D_; cex = 0; }
    else { const int n0 = ct * 224; cb0 = n0 + ((ct & 1) ? 32 : 0); cstep = 64; cex = (ct & 1) ? n0 : n0 + 192; }

    const unsigned lds0 = (unsigned)(uintptr_t)lds;
    const unsigned voffA = (unsigned)((8 * wid + (lane >> 3)) * (D_ * 2) + (((lane & 7) ^ ((4 * wid + (lane >> 4)) & 7)) << 4));
    const unsigned voffB = (unsigned)((8 * wq + (lane >> 3)) * (LDB * 2) + (((lane & 7) ^ ((((lane >> 4) & 1) + 2 * (wq & 1)) << 1)) << 4));
    unsigned voff2 = voffB;
    if (G) {
        if (QKV) voff2 = (unsigned)((4 * wq + ((lane >> 3) & 3)) * (D_ * 2) + (((lane & 7) ^ ((2 * wq + ((lane >> 4) & 1)) & 7)) << 4));
        else voff2 = (unsigned)((8 * wq + ((lane >> 2) & 7)) * (LDB * 2) + ((((lane >> 1) & 1) ^ (wq & 1)) << 5) + ((lane & 1) << 4));
    }
    const char* gA = (const char*)xn + (size_t)m0 * (D_ * 2);
    const char* gB1 = (const char*)w + (size_t)(cb0 + G * cstep) * 2;
    const char* gB2 = G ? (QKV ? (const char*)xn + (size_t)(m0 >= 16 ? m0 - 16 : 0) * (D_ * 2) : (const char*)w + (size_t)cex * 2)
                        : (const char*)w + (size_t)(cb0 + 2 * cstep) * 2;
    constexpr size_t KSB = (size_t)32 * LDB * 2;
    const unsigned dB1 = lds0 + G1_BOFF + G * 4096 + wq * 1024;
    const unsigned dB2 = G ? (QKV ? lds0 + 32768 + wq * 512 : lds0 + G1_BOFF + 12288 + wq * 512) : lds0 + G1_BOFF + 8192 + wq * 1024;
    const bool halo = QKV && G;
    const int lim2 = G ? 32 : 64;
#define STAGE_A(t, kh, ab) do { \
        glds_s(gA + (size_t)(t) * 128 + (size_t)(2 * (kh)) * (64 * D_ * 2), voffA, lds0 + (ab) + (kh) * 16384 + wid * 1024); \
        glds_s(gA + (size_t)(t) * 128 + (size_t)(2 * (kh) + 1) * (64 * D_ * 2), voffA, lds0 + (ab) + (kh) * 16384 + 8192 + wid * 1024); } while (0)
#define STAGE_B(h, s, t, ab) do { \
        glds_s(gB1 + (size_t)(h) * KSB, voffB, dB1 + (s) * G1_BBUF); \
        if (lane < lim2) glds_s(gB2 + (halo ? (size_t)(t) * 128 : (size_t)(h) * KSB), voff2, dB2 + (halo ? (unsigned)(ab) : (unsigned)((s) * G1_BBUF))); } while (0)

    const int q4 = (lane & 15) >> 2, p4 = lane & 3;
    const int aswz = (fq ^ ((fr >> 1) & 7)) << 4;
    const int oA = (wq * 64 + fr) * 128 + aswz, oH = 32768 + fr * 128 + aswz;
    const int bsw = ((q4 >> 1) & 1) + 2 * (fq & 1);
    const int bbase = G1_BOFF + fq * 1024 + q4 * 128 + p4 * 8;
    LAS unsigned char* pB[2] = {lds + bbase + (((2 * G) ^ bsw) << 5), lds + bbase + (((2 * G + 1) ^ bsw) << 5)};
    LAS unsigned char* pEB = lds + G1_BOFF + 12288 + fq * 512 + q4 * 64 + ((G ^ (fq & 1)) << 5) + p4 * 8;

    f32x4 acc[4][6], accE[4];
#pragma unroll
    for (int i = 0; i < 4; ++i) {
        accE[i] = (f32x4){0.f, 0.f, 0.f, 0.f};
#pragma unroll
        for (int c = 0; c < 6; ++c) acc[i][c] = (f32x4){0.f, 0.f, 0.f, 0.f};
    }
    bf16x8 Af[4], Bf[6], Ex;

#define LOADF(s, kh, ab) do { \
        _Pragma("unroll") for (int _c = 0; _c < 6; ++_c) Bf[_c] = lds_tr(pB[_c & 1] + (s) * G1_BBUF + (_c >> 1) * 4096, 512); \
        if (QKV) Ex = lds_b128(lds + (ab) + (oH ^ ((kh) << 6))); else Ex = lds_tr(pEB + (s) * G1_BBUF, 256); \
        _Pragma("unroll") for (int _i = 0; _i < 4; ++_i) Af[_i] = lds_b128(lds + (ab) + (oA ^ ((kh) << 6)) + _i * 2048); \
        SCHED; } while (0)
#define MMA() do { WAIT_L(0); BAR; __builtin_amdgcn_s_setprio(1); \
        _Pragma("unroll") for (int _c = 0; _c < 6; ++_c) _Pragma("unroll") for (int _i = 0; _i < 4; ++_i) \
            acc[_i][_c] = (QKV && _c >= 4) ? mfma16(Af[_i], Bf[_c], acc[_i][_c]) : mfma16(Bf[_c], Af[_i], acc[_i][_c]); \
        if (QKV) { if (wq == 0) { accE[0] = mfma16(Bf[2], Ex, accE[0]); accE[1] = mfma16(Bf[3], Ex, accE[1]); \
                                  accE[2] = mfma16(Ex, Bf[4], accE[2]); accE[3] = mfma16(Ex, Bf[5], accE[3]); } } \
        else { _Pragma("unroll") for (int _i = 0; _i < 4; ++_i) accE[_i] = mfma16(Ex, Af[_i], accE[_i]); } \
        asm volatile("" : "+v"(accE[0]), "+v"(accE[1]), "+v"(accE[2]), "+v"(accE[3])); \
        __builtin_amdgcn_s_setprio(0); BAR; SCHED; } while (0)

    STAMP(0, 0);
    STAGE_A(0, 0, 0); STAGE_A(0, 1, 0); STAGE_B(0, 0, 0, 0);
    STAGE_A(1, 0, G1_ABUF); STAGE_B(1, 1, 1, G1_ABUF);
    STAGE_A(1, 1, G1_ABUF); STAGE_B(2, 2, 1, G1_ABUF);
    WAIT_V(8); BAR;
    if (G == 1) BAR;
    STAMP(0, 1);
    int a0 = 0, a1 = G1_ABUF, a2 = 2 * G1_ABUF;
    for (int t = 0; t < 14; t += 2) {
        const int h = 2 * t;
        LOADF(0, 0, a0); STAGE_A(t + 2, 0, a2); STAGE_B(h + 3, 3, t + 2, a2); WAIT_V(8); MMA();
        LOADF(1, 1, a0); STAGE_A(t + 2, 1, a2); STAGE_B(h + 4, 0, t + 2, a2); WAIT_V(8); MMA();
        LOADF(2, 0, a1); STAGE_A(t + 3, 0, a0); STAGE_B(h + 5, 1, t + 3, a0); WAIT_V(8); MMA();
        LOADF(3, 1, a1); STAGE_A(t + 3, 1, a0); STAGE_B(h + 6, 2, t + 3, a0); WAIT_V(8); MMA();
        const int a = a0; a0 = a2; a2 = a1; a1 = a;
    }
    LOADF(0, 0, a0); STAGE_B(31, 3, 15, a1); WAIT_V(6); MMA();
    LOADF(1, 1, a0);
    glds4_s(pf0, tid * 64, lds0 + G1_PFOFF + wid * 256); glds4_s(pf1, lane * 64, lds0 + G1_PFOFF + wid * 256);
    xb[0] = (u32x4){XB_EMPTY, XB_EMPTY, XB_EMPTY, XB_EMPTY}; xb[512] = (u32x4){XB_EMPTY, XB_EMPTY, XB_EMPTY, XB_EMPTY};
    WAIT_V(6); MMA();
    LOADF(2, 0, a1); WAIT_V(4); MMA();
    LOADF(3, 1, a1); MMA();
    STAMP(0, 2);
#undef STAGE_A
#undef STAGE_B
#undef LOADF
#undef MMA
    const int rt0 = mt * 16 + wq * 4;
    char* gO = (char*)act + lane * 16;
    if (!QKV) {
        if (G == 0) BAR;
#pragma unroll
        for (int i = 0; i < 4; ++i) {
#pragma unroll
            for (int j = 0; j < 3; ++j) {
                const f32x4 a = acc[i][2 * j], b = acc[i][2 * j + 1];
                const u32x4 o = {relu2(pack_bf16(a[0], a[1])), relu2(pack_bf16(a[2], a[3])), relu2(pack_bf16(b[0], b[1])), relu2(pack_bf16(b[2], b[3]))};
                *(u32x4*)(gO + act_block(rt0 + i, ((D_ + cb0) >> 5) + 2 * j + G)) = o;
            }
            const f32x4 e = accE[i];
            const u32x2 o2 = {relu2(pack_bf16(e[0], e[1])), relu2(pack_bf16(e[2], e[3]))};
            *(u32x2*)(gO + act_block(rt0 + i, (D_ + cex) >> 5) + G * 8) = o2;
        }
    } else {
        u32x4 Qf[4], Kf[5], Vf[5];
#pragma unroll
        for (int i = 0; i < 4; ++i) {
            Qf[i] = pack8(acc[i][0], acc[i][1]); Kf[i + 1] = pack8(acc[i][2], acc[i][3]); Vf[i + 1] = pack8(acc[i][4], acc[i][5]);
        }
        LAS u32x4* ex = (LAS u32x4*)(lds + wid * 2048) + lane;
        ex[0] = Kf[4]; ex[64] = Vf[4];
        WAIT_L(0);
        if (G == 0) BAR;
        BAR;
        STAMP(0, 3);
        if (wq == 0) { Kf[0] = pack8(accE[0], accE[1]); Vf[0] = pack8(accE[2], accE[3]); }
        else { Kf[0] = ex[-128]; Vf[0] = ex[-64]; }
        const int par = fr & 1;
        const bool cura = fr - 4 * fq - par >= 0, curb = fr - 4 * fq - par - 2 >= 0;
        const f32x4 z = {0.f, 0.f, 0.f, 0.f};
#pragma unroll
        for (int i = 0; i < 4; ++i) {
            const f32x4 s0 = mfma16(__builtin_bit_cast(bf16x8, Kf[i]), __builtin_bit_cast(bf16x8, Qf[i]), z);
            const f32x4 s1 = mfma16(__builtin_bit_cast(bf16x8, Kf[i + 1]), __builtin_bit_cast(bf16x8, Qf[i]), z);
            float la = cura ? (par ? s1[1] : s1[0]) : (par ? s0[1] : s0[0]);
            float lb = curb ? (par ? s1[3] : s1[2]) : (par ? s0[3] : s0[2]);
            la = rbf(rbf(la) * 0.17677669529663687f); lb = rbf(rbf(lb) * 0.17677669529663687f);
            if (rt0 + i == 0) { la = cura ? la : -1e9f; lb = curb ? lb : -1e9f; }
            const float mx = xred4<true>(fmaxf(la, lb));
            const float ea = __expf(la - mx), eb = __expf(lb - mx);
            const float inv = 1.0f / xred4<false>(ea + eb);
            const unsigned pw = pack_bf16(ea * inv, eb * inv);
            const unsigned wa = (pw & 0xffffu) << (16 * par), wb = (pw >> 16) << (16 * par);
            const u32x4 P = {cura ? 0u : wa, curb ? 0u : wb, cura ? wa : 0u, curb ? wb : 0u};
            const u32x4 V0 = {Vf[i][0], Vf[i][1], Vf[i + 1][0], Vf[i + 1][1]}, V1 = {Vf[i][2], Vf[i][3], Vf[i + 1][2], Vf[i + 1][3]};
            const f32x4 o0 = mfma16(__builtin_bit_cast(bf16x8, V0), __builtin_bit_cast(bf16x8, P), z);
            const f32x4 o1 = mfma16(__builtin_bit_cast(bf16x8, V1), __builtin_bit_cast(bf16x8, P), z);
            *(u32x4*)(gO + act_block(rt0 + i, ct * 2 + G)) = pack8(o0, o1);
        }
    }
    STAMP(0, 4);
    STAMP(0, 5);
}

__global__ __launch_bounds__(512, 2) void gemm1_kernel(const __hip_bfloat16* __restrict__ xn, const __hip_bfloat16* __restrict__ wqkv,
                                                       const __hip_bfloat16* __restrict__ w1, __hip_bfloat16* __restrict__ act,
                                                       const __hip_bfloat16* __restrict__ wo, const __hip_bfloat16* __restrict__ w2,
                                                       unsigned* __restrict__ xbuf) {
    extern __shared__ __attribute__((aligned(16))) unsigned char smem[];
    LAS unsigned char* lds = (LAS unsigned char*)smem;
    const int xcd = blockIdx.x & 7, j = blockIdx.x >> 3;
    const int ct = xcd * 2 + (j & 1), mt = j >> 2;
    const int wid = __builtin_amdgcn_readfirstlane(threadIdx.x >> 6);
    const char* pf0 = blockIdx.x < 224 ? (const char*)w2 + (size_t)blockIdx.x * 32768 : (const char*)wo + (size_t)(blockIdx.x - 224) * 32768;
    const char* pf1 = (const char*)wo + (1u << 20) + (size_t)((blockIdx.x * 8 + wid) & 255) * 4096;
    u32x4* xb = (u32x4*)xbuf + blockIdx.x * 1024 + threadIdx.x;
    if ((j >> 1) & 1) gemm1_body<true>(lds, xn, wqkv, act, pf0, pf1, xb, mt, ct);
    else gemm1_body<false>(lds, xn, w1, act, pf0, pf1, xb, mt, ct);
}

#define PROBE_GAP 4
constexpr int G2_NS = 5, G2_STAGE = 32768, G2_LDS = G2_NS * G2_STAGE, G2_NT = KACT / 64 / 2;
constexpr int G2_XOFF = (G2_NT % G2_NS) * G2_STAGE, G2_ROFF = 65536;
static_assert(G2_XOFF >= 32768 && G2_XOFF + 16384 <= G2_ROFF, "LDS map: probes at 0 and 128 KiB, 32 KiB each");

__global__ __launch_bounds__(768, 3) void gemm2_kernel(const __hip_bfloat16* __restrict__ act, const __hip_bfloat16* __restrict__ wo,
                                                       const __hip_bfloat16* __restrict__ w2, const __hip_bfloat16* __restrict__ x,
                                                       __hip_bfloat16* __restrict__ out, unsigned* __restrict__ xbuf) {
    extern __shared__ __attribute__((aligned(16))) unsigned char smem[];
    LAS unsigned char* lds = (LAS unsigned char*)smem;
    const int tid = threadIdx.x, wid = __builtin_amdgcn_readfirstlane(tid >> 6), lane = tid & 63;
    const int G = wid >> 2, wq = wid & 3, wm = wq >> 1, wn = wq & 1, fr = lane & 15, fq = lane >> 4;
    const int xcd = blockIdx.x & 7, jb = blockIdx.x >> 3;
    const int sp = xcd & 1, mt = (xcd >> 1) * 4 + (jb & 3), nt = jb >> 2;
    const int m0 = mt * 128, n0 = nt * 128, kt0 = sp * G2_NT;
    const int pair = (mt * 8 + nt) * 2;

    if (G == 2) {
        const unsigned voffA = (unsigned)(wq * 1024 + lane * 16);
        const unsigned voffB = (unsigned)((8 * wq + (lane >> 3)) * (D_ * 2) + (((lane & 7) ^ ((lane >> 4) << 1)) << 4));
        const char* gA = (const char*)act + act_block(mt * 8, kt0 * 2);
        const unsigned ldsw = (unsigned)(uintptr_t)lds + wq * 1024;
        auto stage = [&](int t, int slot) {
            const int kt = kt0 + t;
            const char* gB = (kt < 16 ? (const char*)wo + (size_t)kt * (64 * D_ * 2) : (const char*)w2 + (size_t)(kt - 16) * (64 * D_ * 2)) + n0 * 2;
            const unsigned d = ldsw + slot * G2_STAGE;
#pragma unroll
            for (int u = 0; u < 4; ++u) glds_s(gB + (size_t)(u & 1) * (32 * D_ * 2) + (u >> 1) * 128, voffB, d + 16384 + (u >> 1) * 8192 + (u & 1) * 4096);
#pragma unroll
            for (int u = 0; u < 4; ++u) glds_s(gA + (size_t)t * (128 * 2048) + u * 4096, voffA, d + u * 4096);
        };
        for (int t = 0; t < 4; ++t) stage(t, t);
        WAIT_V(24); BAR;
        int ss = 4;
        for (int s = 0; s < G2_NT - 4; ++s) {
            stage(s + 4, ss);
            ss = ss == G2_NS - 1 ? 0 : ss + 1;
            WAIT_V(24); BAR;
        }
        const char* gX = (const char*)x + (size_t)(m0 + (wq >> 1) * 64 + (2 * (wq & 1) + sp) * 16) * (D_ * 2) + n0 * 2;
        const unsigned voffX = (unsigned)((lane >> 4) * (D_ * 2) + (((lane & 15) ^ (lane >> 4)) << 4));
#pragma unroll
        for (int u = 0; u < 4; ++u) glds_s(gX + u * (4 * D_ * 2), voffX ^ (u << 6), (unsigned)(uintptr_t)lds + G2_XOFF + wq * 4096 + u * 1024);
        WAIT_V(20); BAR;
        WAIT_V(12); BAR;
        WAIT_V(4); BAR;
        WAIT_V(0); BAR;
        BAR;
        BAR;
        return;
    }

    const int q4 = (lane & 15) >> 2, p4 = lane & 3;
    const int aoff[2] = {wm * 8192 + lane * 16 + sp * 2048, wm * 8192 + lane * 16 - sp * 2048};
    const int boff = 16384 + wn * 8192 + fq * 512 + q4 * 128 + ((((q4 >> 1) & 1) + 2 * (fq & 1)) << 5) + p4 * 8;
    STAMP(1, 0);
    f32x4 acc[4][4];
#pragma unroll
    for (int i = 0; i < 4; ++i)
#pragma unroll
        for (int c = 0; c < 4; ++c) acc[i][c] = (f32x4){0.f, 0.f, 0.f, 0.f};
    bf16x8 Af[4][2], Bf[4][2];

    BAR;
    if (G == 1) BAR;
    STAMP(1, 1);
    int slot = G;
    auto step = [&]() {
        LAS unsigned char* sb = lds + slot * G2_STAGE;
#pragma unroll
        for (int k = 0; k < 2; ++k) {
#pragma unroll
            for (int c = 0; c < 4; ++c) Bf[c][k] = lds_tr(sb + (boff ^ (c << 5)) + k * 4096, 2048);
#pragma unroll
            for (int i = 0; i < 4; ++i) Af[i][k] = lds_b128(sb + aoff[i & 1] + i * 2048 + k * 1024);
        }
        WAIT_L(0); BAR;
        __builtin_amdgcn_s_setprio(1);
#pragma unroll
        for (int k = 0; k < 2; ++k)
#pragma unroll
            for (int c = 0; c < 4; ++c)
#pragma unroll
                for (int i = 0; i < 4; ++i) acc[i][c] = mfma16(Bf[c][k], Af[i][k], acc[i][c]);
        __builtin_amdgcn_s_setprio(0);
        BAR; SCHED;
        slot += 2; if (slot >= G2_NS) slot -= G2_NS;
    };
#pragma unroll 1
    for (int t = 0; t < G2_NT / 2; ++t) step();
    STAMP(1, 2);
    LAS f32x4* r1 = (LAS f32x4*)(lds + G2_ROFF + wq * 8192) + lane;
    LAS f32x4* r2 = r1 + 2048;
    const int xo = wq * 4096 + lane * 16;
    if (G == 1) {
        const __amdgpu_buffer_rsrc_t rs = __builtin_amdgcn_make_buffer_rsrc((void*)xbuf, 0, 256 * 16384, 0x00020000);
        f32x4 t[8];
#pragma unroll
        for (int e = 0; e < 8; ++e) t[e] = r1[e * 64];
        SCHED;
#pragma unroll
        for (int e = 0; e < 4; ++e)
            __builtin_amdgcn_raw_buffer_store_b128(pack8(acc[(e & 2) + 1][2 * (e & 1)] + t[2 * e], acc[(e & 2) + 1][2 * (e & 1) + 1] + t[2 * e + 1]),
                                                   rs, (pair + sp) * 16384 + xo + e * 1024, 0, 16);
#pragma unroll
        for (int u = 0; u < 2; ++u)
#pragma unroll
            for (int c = 0; c < 4; ++c) r2[(u * 4 + c) * 64] = acc[2 * u][c];
        WAIT_L(0); BAR;
        STAMP(1, 3);
        return;
    }
#pragma unroll
    for (int u = 0; u < 2; ++u)
#pragma unroll
        for (int c = 0; c < 4; ++c) r1[(u * 4 + c) * 64] = acc[2 * u + 1][c];
    WAIT_L(0); BAR;
#define PIN8 asm volatile("" : "+v"(acc[0][0]), "+v"(acc[0][1]), "+v"(acc[0][2]), "+v"(acc[0][3]), "+v"(acc[2][0]), "+v"(acc[2][1]), "+v"(acc[2][2]), "+v"(acc[2][3]))
#pragma unroll
    for (int u = 0; u < 2; ++u)
#pragma unroll
        for (int c = 0; c < 4; ++c) {
            const u32x2 xr = *(LAS u32x2*)(lds + G2_XOFF + (wm * 2 + u) * 4096 + fr * 256 + (((wn * 8 + c * 2 + (fq >> 1)) ^ fr) << 4) + (fq & 1) * 8);
            acc[2 * u][c] += (f32x4){bf_lo(xr[0]), bf_hi(xr[0]), bf_lo(xr[1]), bf_hi(xr[1])};
        }
    PIN8;
    BAR;
    STAMP(1, 3);
    u32x4 rr[4];
    {
        const char* gx = (const char*)xbuf + (size_t)(pair + (sp ^ 1)) * 16384;
        auto probe = [&](int k) {
            const unsigned d = (unsigned)(uintptr_t)lds + (k >> 1) * 131072 + wq * 8192 + (k & 1) * 4096;
#pragma unroll
            for (int e = 0; e < 4; ++e)
                asm volatile("s_mov_b32 m0, %2\n\ts_nop 0\n\tglobal_load_lds_dwordx4 %0, %1 sc1" ::"v"(xo), "s"(gx + e * 1024), "s"(d + e * 1024) : "memory");
        };
        probe(0);
#pragma unroll
        for (int u = 0; u < 2; ++u)
#pragma unroll
            for (int c = 0; c < 4; ++c) acc[2 * u][c] += r2[(u * 4 + c) * 64];
        PIN8;
#pragma unroll
        for (int k = 1; k < 4; ++k) { probe(k); __builtin_amdgcn_s_sleep(PROBE_GAP); }
        for (int spin = 0; spin < (1 << 16); ++spin) {
            const int k = spin & 3;
            WAIT_V(12);
            LAS u32x4* p = (LAS u32x4*)(lds + (k >> 1) * 131072 + wq * 8192 + (k & 1) * 4096) + lane;
            unsigned all = 0u;
#pragma unroll
            for (int e = 0; e < 4; ++e) {
                rr[e] = p[e * 64];
                all = max(max(max(all, rr[e][0]), max(rr[e][1], rr[e][2])), rr[e][3]);
            }
            if (__builtin_amdgcn_ballot_w64(all == XB_EMPTY) == 0) break;
            probe(k);
        }
    }
    STAMP(1, 4);
#pragma unroll
    for (int u = 0; u < 2; ++u) {
        char* gO = (char*)out + (size_t)(m0 + wm * 64 + (2 * u + sp) * 16 + fr) * (D_ * 2) + (n0 + wn * 64 + (fq & 1) * 16 + (fq >> 1) * 8) * 2;
#pragma unroll
        for (int j = 0; j < 2; ++j) {
            const u32x4 r = rr[u * 2 + j];
            const f32x4 a = acc[2 * u][2 * j], b = acc[2 * u][2 * j + 1];
            const auto rx = __builtin_amdgcn_permlane16_swap(pack_bf16(a[0] + bf_lo(r[0]), a[1] + bf_hi(r[0])), pack_bf16(b[0] + bf_lo(r[2]), b[1] + bf_hi(r[2])), false, false);
            const auto ry = __builtin_amdgcn_permlane16_swap(pack_bf16(a[2] + bf_lo(r[1]), a[3] + bf_hi(r[1])), pack_bf16(b[2] + bf_lo(r[3]), b[3] + bf_hi(r[3])), false, false);
            *(u32x4*)(gO + j * 64) = (u32x4){rx[0], ry[0], rx[1], ry[1]};
        }
    }
    STAMP(1, 5);
}

extern "C" void kernel_launch(void* const* d_in, const int* in_sizes, int n_in, void* d_out, int out_size, void* d_ws,
                              size_t ws_size, hipStream_t stream) {
    const __hip_bfloat16* x = (const __hip_bfloat16*)d_in[0];
    const __hip_bfloat16* wqkv = (const __hip_bfloat16*)d_in[1];
    const __hip_bfloat16* wo = (const __hip_bfloat16*)d_in[2];
    const __hip_bfloat16* w1 = (const __hip_bfloat16*)d_in[4];
    const __hip_bfloat16* w2 = (const __hip_bfloat16*)d_in[5];
    char* ws = (char*)d_ws;
    __hip_bfloat16* xn = (__hip_bfloat16*)ws;
    __hip_bfloat16* act = (__hip_bfloat16*)(ws + (4u << 20));
    unsigned* xbuf = (unsigned*)(ws + (24u << 20));
    (void)hipFuncSetAttribute((const void*)gemm1_kernel, hipFuncAttributeMaxDynamicSharedMemorySize, G1_LDS);
    (void)hipFuncSetAttribute((const void*)gemm2_kernel, hipFuncAttributeMaxDynamicSharedMemorySize, G2_LDS);
    ln_kernel<<<S_ / 4, 256, 0, stream>>>(x, xn);
    gemm1_kernel<<<256, 512, G1_LDS, stream>>>(xn, wqkv, w1, act, wo, w2, xbuf);
    gemm2_kernel<<<256, 768, G2_LDS, stream>>>(act, wo, w2, x, (__hip_bfloat16*)d_out, xbuf);
}
```

```cpp
#include <hip/hip_runtime.h>
#include <hip/hip_bf16.h>
#include <stdint.h>

#define LAS __attribute__((address_space(3)))
#define GAS __attribute__((address_space(1)))
typedef __attribute__((ext_vector_type(8))) short bf16x8;
typedef __attribute__((ext_vector_type(4))) short s16x4;
typedef __attribute__((ext_vector_type(4))) float f32x4;
typedef __attribute__((ext_vector_type(2))) float f32x2;
typedef __attribute__((ext_vector_type(2))) __bf16 bf16x2_t;
typedef __attribute__((ext_vector_type(4))) unsigned u32x4;
typedef __attribute__((ext_vector_type(2))) unsigned u32x2;

constexpr int S_ = 2048, D_ = 1024, DFF = 3584, KACT = D_ + DFF;
__device__ __forceinline__ size_t act_block(int rt, int kb) { return (size_t)((kb >> 1) * 128 + rt) * 2048 + (size_t)(kb & 1) * 1024; }

constexpr unsigned XB_EMPTY = 0xffffffffu;

#define WAIT_V(n) asm volatile("s_waitcnt vmcnt(" #n ")" ::: "memory")
#define WAIT_L(n) asm volatile("s_waitcnt lgkmcnt(" #n ")" ::: "memory")
#define BAR do { __builtin_amdgcn_s_barrier(); asm volatile("" ::: "memory"); } while (0)
#define SCHED __builtin_amdgcn_sched_barrier(0)
#define STAMP(k, i)
#define GLDS(src, dst) __builtin_amdgcn_global_load_lds((const unsigned*)(src), (LAS unsigned*)(dst), 16, 0, 0)

__device__ __forceinline__ void glds_s(const void* sbase, unsigned voff, unsigned ldsaddr) {
    asm volatile("s_mov_b32 m0, %2\n\ts_nop 0\n\tglobal_load_lds_dwordx4 %0, %1" ::"v"(voff), "s"(sbase), "s"(ldsaddr) : "memory");
}
__device__ __forceinline__ void glds4_s(const void* sbase, unsigned voff, unsigned ldsaddr) {
    asm volatile("s_mov_b32 m0, %2\n\ts_nop 0\n\tglobal_load_lds_dword %0, %1" ::"v"(voff), "s"(sbase), "s"(ldsaddr) : "memory");
}
__device__ __forceinline__ unsigned pack_bf16(float a, float b) {
    f32x2 v = {a, b};
    bf16x2_t r = __builtin_convertvector(v, bf16x2_t);
    return __builtin_bit_cast(unsigned, r);
}
__device__ __forceinline__ float bf_lo(unsigned u) { return __builtin_bit_cast(float, u << 16); }
__device__ __forceinline__ float bf_hi(unsigned u) { return __builtin_bit_cast(float, u & 0xffff0000u); }
__device__ __forceinline__ float rbf(float a) { return bf_lo(pack_bf16(a, a)); }
__device__ __forceinline__ bf16x8 lds_b128(LAS unsigned char* p) { return *(LAS bf16x8*)p; }
__device__ __forceinline__ bf16x8 lds_tr(LAS unsigned char* p, int hi_off) {
    s16x4 lo = __builtin_amdgcn_ds_read_tr16_b64_v4i16((LAS s16x4*)p);
    s16x4 hi = __builtin_amdgcn_ds_read_tr16_b64_v4i16((LAS s16x4*)(p + hi_off));
    return __builtin_shufflevector(lo, hi, 0, 1, 2, 3, 4, 5, 6, 7);
}
__device__ __forceinline__ f32x4 mfma16(bf16x8 a, bf16x8 b, f32x4 c) {
    return __builtin_amdgcn_mfma_f32_16x16x32_bf16(a, b, c, 0, 0, 0);
}
__device__ __forceinline__ f32x4 sel4(bool c, f32x4 a, f32x4 b) {
    f32x4 r;
#pragma unroll
    for (int e = 0; e < 4; ++e) r[e] = c ? a[e] : b[e];
    return r;
}
__device__ __forceinline__ u32x4 pack8(f32x4 a, f32x4 b) {
    return (u32x4){pack_bf16(a[0], a[1]), pack_bf16(a[2], a[3]), pack_bf16(b[0], b[1]), pack_bf16(b[2], b[3])};
}
__device__ __forceinline__ unsigned relu2(unsigned u) {
    typedef __attribute__((ext_vector_type(2))) short s16x2;
    return __builtin_bit_cast(unsigned, __builtin_elementwise_max(__builtin_bit_cast(s16x2, u), (s16x2){0, 0}));
}
__device__ __forceinline__ float u2f(unsigned u) { return __builtin_bit_cast(float, u); }
__device__ __forceinline__ unsigned f2u(float f) { return __builtin_bit_cast(unsigned, f); }
template <bool MAX>
__device__ __forceinline__ float xred4(float v) {
    const auto a = __builtin_amdgcn_permlane16_swap(f2u(v), f2u(v), false, false);
    v = MAX ? fmaxf(u2f(a[0]), u2f(a[1])) : u2f(a[0]) + u2f(a[1]);
    const auto b = __builtin_amdgcn_permlane32_swap(f2u(v), f2u(v), false, false);
    return MAX ? fmaxf(u2f(b[0]), u2f(b[1])) : u2f(b[0]) + u2f(b[1]);
}

template <int CTRL>
__device__ __forceinline__ float dpp_add(float v) { return v + u2f(__builtin_amdgcn_update_dpp(0u, f2u(v), CTRL, 0xf, 0xf, true)); }
__device__ __forceinline__ float wave_sum(float v) {
    return xred4<false>(dpp_add<0x140>(dpp_add<0x141>(dpp_add<0x4e>(dpp_add<0xb1>(v)))));
}

__global__ __launch_bounds__(256) void ln_kernel(const __hip_bfloat16* __restrict__ x, __hip_bfloat16* __restrict__ xn) {
    const int lane = threadIdx.x & 63, row = blockIdx.x * 4 + (threadIdx.x >> 6);
    const u32x4* src = (const u32x4*)(x + (size_t)row * D_);
    u32x4 v0 = src[lane], v1 = src[64 + lane];
    float f[16];
#pragma unroll
    for (int i = 0; i < 4; ++i) { f[2 * i] = bf_lo(v0[i]); f[2 * i + 1] = bf_hi(v0[i]); f[8 + 2 * i] = bf_lo(v1[i]); f[9 + 2 * i] = bf_hi(v1[i]); }
    float s = 0.f, s1 = 0.f;
#pragma unroll
    for (int i = 0; i < 8; ++i) { s += f[i]; s1 += f[8 + i]; }
    const float mean = wave_sum(s + s1) * (1.0f / D_);
    float q = 0.f, q1 = 0.f;
#pragma unroll
    for (int i = 0; i < 8; ++i) { const float d = f[i] - mean, e = f[8 + i] - mean; q += d * d; q1 += e * e; }
    q = wave_sum(q + q1);
    const float mb = rbf(mean);
    const float ri = 1.0f / rbf(sqrtf(rbf(rbf(q * (1.0f / D_)) + 1e-5f)));
    u32x4 o0, o1;
#pragma unroll
    for (int i = 0; i < 4; ++i) {
        const unsigned d0 = pack_bf16(f[2 * i] - mb, f[2 * i + 1] - mb), d1 = pack_bf16(f[8 + 2 * i] - mb, f[9 + 2 * i] - mb);
        o0[i] = pack_bf16(bf_lo(d0) * ri, bf_hi(d0) * ri);
        o1[i] = pack_bf16(bf_lo(d1) * ri, bf_hi(d1) * ri);
    }
    u32x4* dst = (u32x4*)(xn + (size_t)row * D_);
    dst[lane] = o0; dst[64 + lane] = o1;
}

constexpr int G1_ABUF = 34816, G1_BOFF = 3 * G1_ABUF, G1_BBUF = 14336, G1_PFOFF = G1_BOFF + 4 * G1_BBUF, G1_LDS = G1_PFOFF + 2048;

template <bool QKV>
__device__ __forceinline__ void gemm1_body(LAS unsigned char* lds, const __hip_bfloat16* __restrict__ xn,
                                           const __hip_bfloat16* __restrict__ w, __hip_bfloat16* __restrict__ act,
                                           const char* __restrict__ pf0, const char* __restrict__ pf1, u32x4* __restrict__ xb,
                                           const int mt, const int ct) {
    constexpr int LDB = QKV ? 3 * D_ : DFF;
    const int tid = threadIdx.x, wid = __builtin_amdgcn_readfirstlane(tid >> 6), lane = tid & 63;
    const int G = wid >> 2, wq = wid & 3, fr = lane & 15, fq = lane >> 4;
    const int m0 = mt * 256;
    int cb0, cstep, cex;
    if (QKV) { cb0 = ct * 64; cstep = D_; cex = 0; }
    else { const int n0 = ct * 224; cb0 = n0 + ((ct & 1) ? 32 : 0); cstep = 64; cex = (ct & 1) ? n0 : n0 + 192; }

    const unsigned lds0 = (unsigned)(uintptr_t)lds;
    const unsigned voffA = (unsigned)((8 * wid + (lane >> 3)) * (D_ * 2) + (((lane & 7) ^ ((4 * wid + (lane >> 4)) & 7)) << 4));
    const unsigned voffB = (unsigned)((8 * wq + (lane >> 3)) * (LDB * 2) + (((lane & 7) ^ ((((lane >> 4) & 1) + 2 * (wq & 1)) << 1)) << 4));
    unsigned voff2 = voffB;
    if (G) {
        if (QKV) voff2 = (unsigned)((4 * wq + ((lane >> 3) & 3)) * (D_ * 2) + (((lane & 7) ^ ((2 * wq + ((lane >> 4) & 1)) & 7)) << 4));
        else voff2 = (unsigned)((8 * wq + ((lane >> 2) & 7)) * (LDB * 2) + ((((lane >> 1) & 1) ^ (wq & 1)) << 5) + ((lane & 1) << 4));
    }
    const char* gA = (const char*)xn + (size_t)m0 * (D_ * 2);
    const char* gB1 = (const char*)w + (size_t)(cb0 + G * cstep) * 2;
    const char* gB2 = G ? (QKV ? (const char*)xn + (size_t)(m0 >= 16 ? m0 - 16 : 0) * (D_ * 2) : (const char*)w + (size_t)cex * 2)
                        : (const char*)w + (size_t)(cb0 + 2 * cstep) * 2;
    constexpr size_t KSB = (size_t)32 * LDB * 2;
    const unsigned dB1 = lds0 + G1_BOFF + G * 4096 + wq * 1024;
    const unsigned dB2 = G ? (QKV ? lds0 + 32768 + wq * 512 : lds0 + G1_BOFF + 12288 + wq * 512) : lds0 + G1_BOFF + 8192 + wq * 1024;
    const bool halo = QKV && G;
    const int lim2 = G ? 32 : 64;
#define STAGE_A(t, kh, ab) do { \
        glds_s(gA + (size_t)(t) * 128 + (size_t)(2 * (kh)) * (64 * D_ * 2), voffA, lds0 + (ab) + (kh) * 16384 + wid * 1024); \
        glds_s(gA + (size_t)(t) * 128 + (size_t)(2 * (kh) + 1) * (64 * D_ * 2), voffA, lds0 + (ab) + (kh) * 16384 + 8192 + wid * 1024); } while (0)
#define STAGE_B(h, s, t, ab) do { \
        glds_s(gB1 + (size_t)(h) * KSB, voffB, dB1 + (s) * G1_BBUF); \
        if (lane < lim2) glds_s(gB2 + (halo ? (size_t)(t) * 128 : (size_t)(h) * KSB), voff2, dB2 + (halo ? (unsigned)(ab) : (unsigned)((s) * G1_BBUF))); } while (0)

    const int q4 = (lane & 15) >> 2, p4 = lane & 3;
    const int aswz = (fq ^ ((fr >> 1) & 7)) << 4;
    const int oA = (wq * 64 + fr) * 128 + aswz, oH = 32768 + fr * 128 + aswz;
    const int bsw = ((q4 >> 1) & 1) + 2 * (fq & 1);
    const int bbase = G1_BOFF + fq * 1024 + q4 * 128 + p4 * 8;
    LAS unsigned char* pB[2] = {lds + bbase + (((2 * G) ^ bsw) << 5), lds + bbase + (((2 * G + 1) ^ bsw) << 5)};
    LAS unsigned char* pEB = lds + G1_BOFF + 12288 + fq * 512 + q4 * 64 + ((G ^ (fq & 1)) << 5) + p4 * 8;

    f32x4 acc[4][6], accE[4];
#pragma unroll
    for (int i = 0; i < 4; ++i) {
        accE[i] = (f32x4){0.f, 0.f, 0.f, 0.f};
#pragma unroll
        for (int c = 0; c < 6; ++c) acc[i][c] = (f32x4){0.f, 0.f, 0.f, 0.f};
    }
    bf16x8 Af[4], Bf[6], Ex;

#define LOADF(s, kh, ab) do { \
        _Pragma("unroll") for (int _c = 0; _c < 6; ++_c) Bf[_c] = lds_tr(pB[_c & 1] + (s) * G1_BBUF + (_c >> 1) * 4096, 512); \
        if (QKV) Ex = lds_b128(lds + (ab) + (oH ^ ((kh) << 6))); else Ex = lds_tr(pEB + (s) * G1_BBUF, 256); \
        _Pragma("unroll") for (int _i = 0; _i < 4; ++_i) Af[_i] = lds_b128(lds + (ab) + (oA ^ ((kh) << 6)) + _i * 2048); \
        SCHED; } while (0)
#define MMA() do { WAIT_L(0); BAR; __builtin_amdgcn_s_setprio(1); \
        _Pragma("unroll") for (int _c = 0; _c < 6; ++_c) _Pragma("unroll") for (int _i = 0; _i < 4; ++_i) \
            acc[_i][_c] = (QKV && _c >= 4) ? mfma16(Af[_i], Bf[_c], acc[_i][_c]) : mfma16(Bf[_c], Af[_i], acc[_i][_c]); \
        if (QKV) { if (wq == 0) { accE[0] = mfma16(Bf[2], Ex, accE[0]); accE[1] = mfma16(Bf[3], Ex, accE[1]); \
                                  accE[2] = mfma16(Ex, Bf[4], accE[2]); accE[3] = mfma16(Ex, Bf[5], accE[3]); } } \
        else { _Pragma("unroll") for (int _i = 0; _i < 4; ++_i) accE[_i] = mfma16(Ex, Af[_i], accE[_i]); } \
        asm volatile("" : "+v"(accE[0]), "+v"(accE[1]), "+v"(accE[2]), "+v"(accE[3])); \
        __builtin_amdgcn_s_setprio(0); BAR; SCHED; } while (0)

    STAMP(0, 0);
    STAGE_A(0, 0, 0); STAGE_A(0, 1, 0); STAGE_B(0, 0, 0, 0);
    STAGE_A(1, 0, G1_ABUF); STAGE_B(1, 1, 1, G1_ABUF);
    STAGE_A(1, 1, G1_ABUF); STAGE_B(2, 2, 1, G1_ABUF);
    WAIT_V(8); BAR;
    if (G == 1) BAR;
    STAMP(0, 1);
    int a0 = 0, a1 = G1_ABUF, a2 = 2 * G1_ABUF;
    for (int t = 0; t < 14; t += 2) {
        const int h = 2 * t;
        LOADF(0, 0, a0); STAGE_A(t + 2, 0, a2); STAGE_B(h + 3, 3, t + 2, a2); WAIT_V(8); MMA();
        LOADF(1, 1, a0); STAGE_A(t + 2, 1, a2); STAGE_B(h + 4, 0, t + 2, a2); WAIT_V(8); MMA();
        LOADF(2, 0, a1); STAGE_A(t + 3, 0, a0); STAGE_B(h + 5, 1, t + 3, a0); WAIT_V(8); MMA();
        LOADF(3, 1, a1); STAGE_A(t + 3, 1, a0); STAGE_B(h + 6, 2, t + 3, a0); WAIT_V(8); MMA();
        const int a = a0; a0 = a2; a2 = a1; a1 = a;
    }
    LOADF(0, 0, a0); STAGE_B(31, 3, 15, a1); WAIT_V(6); MMA();
    LOADF(1, 1, a0);
    glds4_s(pf0, tid * 64, lds0 + G1_PFOFF + wid * 256); glds4_s(pf1, lane * 64, lds0 + G1_PFOFF + wid * 256);
    xb[0] = (u32x4){XB_EMPTY, XB_EMPTY, XB_EMPTY, XB_EMPTY}; xb[512] = (u32x4){XB_EMPTY, XB_EMPTY, XB_EMPTY, XB_EMPTY};
    WAIT_V(6); MMA();
    LOADF(2, 0, a1); WAIT_V(4); MMA();
    LOADF(3, 1, a1); MMA();
    STAMP(0, 2);
#undef STAGE_A
#undef STAGE_B
#undef LOADF
#undef MMA
    const int rt0 = mt * 16 + wq * 4;
    char* gO = (char*)act + lane * 16;
    if (!QKV) {
        if (G == 0) BAR;
#pragma unroll
        for (int i = 0; i < 4; ++i) {
#pragma unroll
            for (int j = 0; j < 3; ++j) {
                const f32x4 a = acc[i][2 * j], b = acc[i][2 * j + 1];
                const u32x4 o = {relu2(pack_bf16(a[0], a[1])), relu2(pack_bf16(a[2], a[3])), relu2(pack_bf16(b[0], b[1])), relu2(pack_bf16(b[2], b[3]))};
                *(u32x4*)(gO + act_block(rt0 + i, ((D_ + cb0) >> 5) + 2 * j + G)) = o;
            }
            const f32x4 e = accE[i];
            const u32x2 o2 = {relu2(pack_bf16(e[0], e[1])), relu2(pack_bf16(e[2], e[3]))};
            *(u32x2*)(gO + act_block(rt0 + i, (D_ + cex) >> 5) + G * 8) = o2;
        }
    } else {
        u32x4 Qf[4], Kf[5], Vf[5];
#pragma unroll
        for (int i = 0; i < 4; ++i) {
            Qf[i] = pack8(acc[i][0], acc[i][1]); Kf[i + 1] = pack8(acc[i][2], acc[i][3]); Vf[i + 1] = pack8(acc[i][4], acc[i][5]);
        }
        LAS u32x4* ex = (LAS u32x4*)(lds + wid * 2048) + lane;
        ex[0] = Kf[4]; ex[64] = Vf[4];
        WAIT_L(0);
        if (G == 0) BAR;
        BAR;
        STAMP(0, 3);
        if (wq == 0) { Kf[0] = pack8(accE[0], accE[1]); Vf[0] = pack8(accE[2], accE[3]); }
        else { Kf[0] = ex[-128]; Vf[0] = ex[-64]; }
        const int par = fr & 1;
        const bool cura = fr - 4 * fq - par >= 0, curb = fr - 4 * fq - par - 2 >= 0;
        const f32x4 z = {0.f, 0.f, 0.f, 0.f};
#pragma unroll
        for (int i = 0; i < 4; ++i) {
            const f32x4 s0 = mfma16(__builtin_bit_cast(bf16x8, Kf[i]), __builtin_bit_cast(bf16x8, Qf[i]), z);
            const f32x4 s1 = mfma16(__builtin_bit_cast(bf16x8, Kf[i + 1]), __builtin_bit_cast(bf16x8, Qf[i]), z);
            float la = cura ? (par ? s1[1] : s1[0]) : (par ? s0[1] : s0[0]);
            float lb = curb ? (par ? s1[3] : s1[2]) : (par ? s0[3] : s0[2]);
            la = rbf(rbf(la) * 0.17677669529663687f); lb = rbf(rbf(lb) * 0.17677669529663687f);
            if (rt0 + i == 0) { la = cura ? la : -1e9f; lb = curb ? lb : -1e9f; }
            const float mx = xred4<true>(fmaxf(la, lb));
            const float ea = __expf(la - mx), eb = __expf(lb - mx);
            const float inv = 1.0f / xred4<false>(ea + eb);
            const unsigned pw = pack_bf16(ea * inv, eb * inv);
            const unsigned wa = (pw & 0xffffu) << (16 * par), wb = (pw >> 16) << (16 * par);
            const u32x4 P = {cura ? 0u : wa, curb ? 0u : wb, cura ? wa : 0u, curb ? wb : 0u};
            const u32x4 V0 = {Vf[i][0], Vf[i][1], Vf[i + 1][0], Vf[i + 1][1]}, V1 = {Vf[i][2], Vf[i][3], Vf[i + 1][2], Vf[i + 1][3]};
            const f32x4 o0 = mfma16(__builtin_bit_cast(bf16x8, V0), __builtin_bit_cast(bf16x8, P), z);
            const f32x4 o1 = mfma16(__builtin_bit_cast(bf16x8, V1), __builtin_bit_cast(bf16x8, P), z);
            *(u32x4*)(gO + act_block(rt0 + i, ct * 2 + G)) = pack8(o0, o1);
        }
    }
    STAMP(0, 4);
    STAMP(0, 5);
}

__global__ __launch_bounds__(512, 2) void gemm1_kernel(const __hip_bfloat16* __restrict__ xn, const __hip_bfloat16* __restrict__ wqkv,
                                                       const __hip_bfloat16* __restrict__ w1, __hip_bfloat16* __restrict__ act,
                                                       const __hip_bfloat16* __restrict__ wo, const __hip_bfloat16* __restrict__ w2,
                                                       unsigned* __restrict__ xbuf) {
    extern __shared__ __attribute__((aligned(16))) unsigned char smem[];
    LAS unsigned char* lds = (LAS unsigned char*)smem;
    const int xcd = blockIdx.x & 7, j = blockIdx.x >> 3;
    const int ct = xcd * 2 + (j & 1), mt = j >> 2;
    const int wid = __builtin_amdgcn_readfirstlane(threadIdx.x >> 6);
    const char* pf0 = blockIdx.x < 224 ? (const char*)w2 + (size_t)blockIdx.x * 32768 : (const char*)wo + (size_t)(blockIdx.x - 224) * 32768;
    const char* pf1 = (const char*)wo + (1u << 20) + (size_t)((blockIdx.x * 8 + wid) & 255) * 4096;
    u32x4* xb = (u32x4*)xbuf + blockIdx.x * 1024 + threadIdx.x;
    if ((j >> 1) & 1) gemm1_body<true>(lds, xn, wqkv, act, pf0, pf1, xb, mt, ct);
    else gemm1_body<false>(lds, xn, w1, act, pf0, pf1, xb, mt, ct);
}

#define PROBE_GAP 4
constexpr int G2_NS = 5, G2_STAGE = 32768, G2_LDS = G2_NS * G2_STAGE, G2_NT = KACT / 64 / 2;
constexpr int G2_XOFF = (G2_NT % G2_NS) * G2_STAGE, G2_ROFF = 65536;
static_assert(G2_XOFF >= 32768 && G2_XOFF + 16384 <= G2_ROFF, "LDS map: probes at 0 and 128 KiB, 32 KiB each");

__global__ __launch_bounds__(768, 3) void gemm2_kernel(const __hip_bfloat16* __restrict__ act, const __hip_bfloat16* __restrict__ wo,
                                                       const __hip_bfloat16* __restrict__ w2, const __hip_bfloat16* __restrict__ x,
                                                       __hip_bfloat16* __restrict__ out, unsigned* __restrict__ xbuf) {
    extern __shared__ __attribute__((aligned(16))) unsigned char smem[];
    LAS unsigned char* lds = (LAS unsigned char*)smem;
    const int tid = threadIdx.x, wid = __builtin_amdgcn_readfirstlane(tid >> 6), lane = tid & 63;
    const int G = wid >> 2, wq = wid & 3, wm = wq >> 1, wn = wq & 1, fr = lane & 15, fq = lane >> 4;
    const int xcd = blockIdx.x & 7, jb = blockIdx.x >> 3;
    const int sp = xcd & 1, mt = (xcd >> 1) * 4 + (jb & 3), nt = jb >> 2;
    const int m0 = mt * 128, n0 = nt * 128, kt0 = sp * G2_NT;
    const int pair = (mt * 8 + nt) * 2;

    if (G == 2) {
        const unsigned voffA = (unsigned)(wq * 1024 + lane * 16);
        const unsigned voffB = (unsigned)((8 * wq + (lane >> 3)) * (D_ * 2) + (((lane & 7) ^ ((lane >> 4) << 1)) << 4));
        const char* gA = (const char*)act + act_block(mt * 8, kt0 * 2);
        const unsigned ldsw = (unsigned)(uintptr_t)lds + wq * 1024;
        auto stage = [&](int t, int slot) {
            const int kt = kt0 + t;
            const char* gB = (kt < 16 ? (const char*)wo + (size_t)kt * (64 * D_ * 2) : (const char*)w2 + (size_t)(kt - 16) * (64 * D_ * 2)) + n0 * 2;
            const unsigned d = ldsw + slot * G2_STAGE;
#pragma unroll
            for (int u = 0; u < 4; ++u) glds_s(gB + (size_t)(u & 1) * (32 * D_ * 2) + (u >> 1) * 128, voffB, d + 16384 + (u >> 1) * 8192 + (u & 1) * 4096);
#pragma unroll
            for (int u = 0; u < 4; ++u) glds_s(gA + (size_t)t * (128 * 2048) + u * 4096, voffA, d + u * 4096);
        };
        for (int t = 0; t < 4; ++t) stage(t, t);
        WAIT_V(24); BAR;
        int ss = 4;
        for (int s = 0; s < G2_NT - 4; ++s) {
            stage(s + 4, ss);
            ss = ss == G2_NS - 1 ? 0 : ss + 1;
            WAIT_V(24); BAR;
        }
        const char* gX = (const char*)x + (size_t)(m0 + (wq >> 1) * 64 + (2 * (wq & 1) + sp) * 16) * (D_ * 2) + n0 * 2;
        const unsigned voffX = (unsigned)((lane >> 4) * (D_ * 2) + (((lane & 15) ^ (lane >> 4)) << 4));
#pragma unroll
        for (int u = 0; u < 4; ++u) glds_s(gX + u * (4 * D_ * 2), voffX ^ (u << 6), (unsigned)(uintptr_t)lds + G2_XOFF + wq * 4096 + u * 1024);
        WAIT_V(20); BAR;
        WAIT_V(12); BAR;
        WAIT_V(4); BAR;
        BAR; BAR;
        WAIT_V(0); BAR;
        return;
    }

    const int q4 = (lane & 15) >> 2, p4 = lane & 3;
    const int aoff[2] = {wm * 8192 + lane * 16 + sp * 2048, wm * 8192 + lane * 16 - sp * 2048};
    const int boff = 16384 + wn * 8192 + fq * 512 + q4 * 128 + ((((q4 >> 1) & 1) + 2 * (fq & 1)) << 5) + p4 * 8;
    STAMP(1, 0);
    f32x4 acc[4][4];
#pragma unroll
    for (int i = 0; i < 4; ++i)
#pragma unroll
        for (int c = 0; c < 4; ++c) acc[i][c] = (f32x4){0.f, 0.f, 0.f, 0.f};
    bf16x8 Af[4][2], Bf[4][2];

    BAR;
    if (G == 1) BAR;
    STAMP(1, 1);
    int slot = G;
    auto step = [&]() {
        LAS unsigned char* sb = lds + slot * G2_STAGE;
#pragma unroll
        for (int k = 0; k < 2; ++k) {
#pragma unroll
            for (int c = 0; c < 4; ++c) Bf[c][k] = lds_tr(sb + (boff ^ (c << 5)) + k * 4096, 2048);
#pragma unroll
            for (int i = 0; i < 4; ++i) Af[i][k] = lds_b128(sb + aoff[i & 1] + i * 2048 + k * 1024);
        }
        WAIT_L(0); BAR;
        __builtin_amdgcn_s_setprio(1);
#pragma unroll
        for (int k = 0; k < 2; ++k)
#pragma unroll
            for (int c = 0; c < 4; ++c)
#pragma unroll
                for (int i = 0; i < 4; ++i) acc[i][c] = mfma16(Bf[c][k], Af[i][k], acc[i][c]);
        __builtin_amdgcn_s_setprio(0);
        BAR; SCHED;
        slot += 2; if (slot >= G2_NS) slot -= G2_NS;
    };
#pragma unroll 1
    for (int t = 0; t < G2_NT / 2; ++t) step();
    STAMP(1, 2);
    LAS f32x4* r1 = (LAS f32x4*)(lds + G2_ROFF + wq * 8192) + lane;
    LAS f32x4* r2 = r1 + 2048;
    const int xo = wq * 4096 + lane * 16;
    if (G == 1) {
        const __amdgpu_buffer_rsrc_t rs = __builtin_amdgcn_make_buffer_rsrc((void*)xbuf, 0, 256 * 16384, 0x00020000);
        f32x4 t[8];
#pragma unroll
        for (int e = 0; e < 8; ++e) t[e] = r1[e * 64];
        SCHED;
#pragma unroll
        for (int e = 0; e < 4; ++e)
            __builtin_amdgcn_raw_buffer_store_b128(pack8(acc[(e & 2) + 1][2 * (e & 1)] + t[2 * e], acc[(e & 2) + 1][2 * (e & 1) + 1] + t[2 * e + 1]),
                                                   rs, (pair + sp) * 16384 + xo + e * 1024, 0, 16);
#pragma unroll
        for (int u = 0; u < 2; ++u)
#pragma unroll
            for (int c = 0; c < 4; ++c) r2[(u * 4 + c) * 64] = acc[2 * u][c];
        WAIT_L(0); BAR;
        STAMP(1, 3);
        return;
    }
#pragma unroll
    for (int u = 0; u < 2; ++u)
#pragma unroll
        for (int c = 0; c < 4; ++c) r1[(u * 4 + c) * 64] = acc[2 * u + 1][c];
    WAIT_L(0); BAR;
#define PIN8 asm volatile("" : "+v"(acc[0][0]), "+v"(acc[0][1]), "+v"(acc[0][2]), "+v"(acc[0][3]), "+v"(acc[2][0]), "+v"(acc[2][1]), "+v"(acc[2][2]), "+v"(acc[2][3]))
    BAR;
    STAMP(1, 3);
    u32x4 rr[4];
    {
        const char* gx = (const char*)xbuf + (size_t)(pair + (sp ^ 1)) * 16384;
        auto probe = [&](int k) {
            const unsigned d = (unsigned)(uintptr_t)lds + (k >> 1) * 131072 + wq * 8192 + (k & 1) * 4096;
#pragma unroll
            for (int e = 0; e < 4; ++e)
                asm volatile("s_mov_b32 m0, %2\n\ts_nop 0\n\tglobal_load_lds_dwordx4 %0, %1 sc1" ::"v"(xo), "s"(gx + e * 1024), "s"(d + e * 1024) : "memory");
        };
        probe(0);
#pragma unroll
        for (int u = 0; u < 2; ++u)
#pragma unroll
            for (int c = 0; c < 4; ++c) {
                const u32x2 xr = *(LAS u32x2*)(lds + G2_XOFF + (wm * 2 + u) * 4096 + fr * 256 + (((wn * 8 + c * 2 + (fq >> 1)) ^ fr) << 4) + (fq & 1) * 8);
                acc[2 * u][c] += r2[(u * 4 + c) * 64] + (f32x4){bf_lo(xr[0]), bf_hi(xr[0]), bf_lo(xr[1]), bf_hi(xr[1])};
            }
        PIN8;
#pragma unroll
        for (int k = 1; k < 4; ++k) { probe(k); __builtin_amdgcn_s_sleep(PROBE_GAP); }
        for (int spin = 0; spin < (1 << 16); ++spin) {
            const int k = spin & 3;
            WAIT_V(12);
            LAS u32x4* p = (LAS u32x4*)(lds + (k >> 1) * 131072 + wq * 8192 + (k & 1) * 4096) + lane;
            unsigned all = 0u;
#pragma unroll
            for (int e = 0; e < 4; ++e) {
                rr[e] = p[e * 64];
                all = max(max(max(all, rr[e][0]), max(rr[e][1], rr[e][2])), rr[e][3]);
            }
            if (__builtin_amdgcn_ballot_w64(all == XB_EMPTY) == 0) break;
            probe(k);
        }
    }
    STAMP(1, 4);
#pragma unroll
    for (int u = 0; u < 2; ++u) {
        char* gO = (char*)out + (size_t)(m0 + wm * 64 + (2 * u + sp) * 16 + fr) * (D_ * 2) + (n0 + wn * 64 + (fq & 1) * 16 + (fq >> 1) * 8) * 2;
#pragma unroll
        for (int j = 0; j < 2; ++j) {
            const u32x4 r = rr[u * 2 + j];
            const f32x4 a = acc[2 * u][2 * j], b = acc[2 * u][2 * j + 1];
            const auto rx = __builtin_amdgcn_permlane16_swap(pack_bf16(a[0] + bf_lo(r[0]), a[1] + bf_hi(r[0])), pack_bf16(b[0] + bf_lo(r[2]), b[1] + bf_hi(r[2])), false, false);
            const auto ry = __builtin_amdgcn_permlane16_swap(pack_bf16(a[2] + bf_lo(r[1]), a[3] + bf_hi(r[1])), pack_bf16(b[2] + bf_lo(r[3]), b[3] + bf_hi(r[3])), false, false);
            *(u32x4*)(gO + j * 64) = (u32x4){rx[0], ry[0], rx[1], ry[1]};
        }
    }
    STAMP(1, 5);
}

extern "C" void kernel_launch(void* const* d_in, const int* in_sizes, int n_in, void* d_out, int out_size, void* d_ws,
                              size_t ws_size, hipStream_t stream) {
    const __hip_bfloat16* x = (const __hip_bfloat16*)d_in[0];
    const __hip_bfloat16* wqkv = (const __hip_bfloat16*)d_in[1];
    const __hip_bfloat16* wo = (const __hip_bfloat16*)d_in[2];
    const __hip_bfloat16* w1 = (const __hip_bfloat16*)d_in[4];
    const __hip_bfloat16* w2 = (const __hip_bfloat16*)d_in[5];
    char* ws = (char*)d_ws;
    __hip_bfloat16* xn = (__hip_bfloat16*)ws;
    __hip_bfloat16* act = (__hip_bfloat16*)(ws + (4u << 20));
    unsigned* xbuf = (unsigned*)(ws + (24u << 20));
    (void)hipFuncSetAttribute((const void*)gemm1_kernel, hipFuncAttributeMaxDynamicSharedMemorySize, G1_LDS);
    (void)hipFuncSetAttribute((const void*)gemm2_kernel, hipFuncAttributeMaxDynamicSharedMemorySize, G2_LDS);
    ln_kernel<<<S_ / 4, 256, 0, stream>>>(x, xn);
    gemm1_kernel<<<256, 512, G1_LDS, stream>>>(xn, wqkv, w1, act, wo, w2, xbuf);
    gemm2_kernel<<<256, 768, G2_LDS, stream>>>(act, wo, w2, x, (__hip_bfloat16*)d_out, xbuf);
}
```

```cpp
#include <hip/hip_runtime.h>
#include <hip/hip_bf16.h>
#include <stdint.h>

#define LAS __attribute__((address_space(3)))
#define GAS __attribute__((address_space(1)))
typedef __attribute__((ext_vector_type(8))) short bf16x8;
typedef __attribute__((ext_vector_type(4))) short s16x4;
typedef __attribute__((ext_vector_type(4))) float f32x4;
typedef __attribute__((ext_vector_type(2))) float f32x2;
typedef __attribute__((ext_vector_type(2))) __bf16 bf16x2_t;
typedef __attribute__((ext_vector_type(4))) unsigned u32x4;
typedef __attribute__((ext_vector_type(2))) unsigned u32x2;

constexpr int S_ = 2048, D_ = 1024, DFF = 3584, KACT = D_ + DFF;
__device__ __forceinline__ size_t act_block(int rt, int kb) { return (size_t)((kb >> 1) * 128 + rt) * 2048 + (size_t)(kb & 1) * 1024; }

constexpr unsigned XB_EMPTY = 0xffffffffu;

#define WAIT_V(n) asm volatile("s_waitcnt vmcnt(" #n ")" ::: "memory")
#define WAIT_L(n) asm volatile("s_waitcnt lgkmcnt(" #n ")" ::: "memory")
#define BAR do { __builtin_amdgcn_s_barrier(); asm volatile("" ::: "memory"); } while (0)
#define SCHED __builtin_amdgcn_sched_barrier(0)
#define STAMP(k, i)
#define GLDS(src, dst) __builtin_amdgcn_global_load_lds((const unsigned*)(src), (LAS unsigned*)(dst), 16, 0, 0)

__device__ __forceinline__ void glds_s(const void* sbase, unsigned voff, unsigned ldsaddr) {
    asm volatile("s_mov_b32 m0, %2\n\ts_nop 0\n\tglobal_load_lds_dwordx4 %0, %1" ::"v"(voff), "s"(sbase), "s"(ldsaddr) : "memory");
}
__device__ __forceinline__ void glds4_s(const void* sbase, unsigned voff, unsigned ldsaddr) {
    asm volatile("s_mov_b32 m0, %2\n\ts_nop 0\n\tglobal_load_lds_dword %0, %1" ::"v"(voff), "s"(sbase), "s"(ldsaddr) : "memory");
}
__device__ __forceinline__ unsigned pack_bf16(float a, float b) {
    f32x2 v = {a, b};
    bf16x2_t r = __builtin_convertvector(v, bf16x2_t);
    return __builtin_bit_cast(unsigned, r);
}
__device__ __forceinline__ float bf_lo(unsigned u) { return __builtin_bit_cast(float, u << 16); }
__device__ __forceinline__ float bf_hi(unsigned u) { return __builtin_bit_cast(float, u & 0xffff0000u); }
__device__ __forceinline__ float rbf(float a) { return bf_lo(pack_bf16(a, a)); }
__device__ __forceinline__ bf16x8 lds_b128(LAS unsigned char* p) { return *(LAS bf16x8*)p; }
__device__ __forceinline__ bf16x8 lds_tr(LAS unsigned char* p, int hi_off) {
    s16x4 lo = __builtin_amdgcn_ds_read_tr16_b64_v4i16((LAS s16x4*)p);
    s16x4 hi = __builtin_amdgcn_ds_read_tr16_b64_v4i16((LAS s16x4*)(p + hi_off));
    return __builtin_shufflevector(lo, hi, 0, 1, 2, 3, 4, 5, 6, 7);
}
__device__ __forceinline__ f32x4 mfma16(bf16x8 a, bf16x8 b, f32x4 c) {
    return __builtin_amdgcn_mfma_f32_16x16x32_bf16(a, b, c, 0, 0, 0);
}
__device__ __forceinline__ f32x4 sel4(bool c, f32x4 a, f32x4 b) {
    f32x4 r;
#pragma unroll
    for (int e = 0; e < 4; ++e) r[e] = c ? a[e] : b[e];
    return r;
}
__device__ __forceinline__ u32x4 pack8(f32x4 a, f32x4 b) {
    return (u32x4){pack_bf16(a[0], a[1]), pack_bf16(a[2], a[3]), pack_bf16(b[0], b[1]), pack_bf16(b[2], b[3])};
}
__device__ __forceinline__ unsigned relu2(unsigned u) {
    typedef __attribute__((ext_vector_type(2))) short s16x2;
    return __builtin_bit_cast(unsigned, __builtin_elementwise_max(__builtin_bit_cast(s16x2, u), (s16x2){0, 0}));
}
#define ST_MODE 3
#define ST_POL " sc0 sc1"
__device__ __forceinline__ void st16(void* p, u32x4 v) { asm volatile("global_store_dwordx4 %0, %1, off" ST_POL "\n\ts_nop 1" ::"v"(p), "v"(v) : "memory"); }
__device__ __forceinline__ void st8(void* p, u32x2 v) { asm volatile("global_store_dwordx2 %0, %1, off" ST_POL ::"v"(p), "v"(v) : "memory"); }
__device__ __forceinline__ float u2f(unsigned u) { return __builtin_bit_cast(float, u); }
__device__ __forceinline__ unsigned f2u(float f) { return __builtin_bit_cast(unsigned, f); }
__device__ __forceinline__ float vmax(float a, float b) { float r; asm("v_max_f32 %0, %1, %2" : "=v"(r) : "v"(a), "v"(b)); return r; }
template <bool MAX>
__device__ __forceinline__ float xred4(float v) {
    const auto a = __builtin_amdgcn_permlane16_swap(f2u(v), f2u(v), false, false);
    v = MAX ? vmax(u2f(a[0]), u2f(a[1])) : u2f(a[0]) + u2f(a[1]);
    const auto b = __builtin_amdgcn_permlane32_swap(f2u(v), f2u(v), false, false);
    return MAX ? vmax(u2f(b[0]), u2f(b[1])) : u2f(b[0]) + u2f(b[1]);
}

template <int CTRL>
__device__ __forceinline__ float dpp_add(float v) { return v + u2f(__builtin_amdgcn_update_dpp(0u, f2u(v), CTRL, 0xf, 0xf, true)); }
__device__ __forceinline__ float wave_sum(float v) {
    return xred4<false>(dpp_add<0x140>(dpp_add<0x141>(dpp_add<0x4e>(dpp_add<0xb1>(v)))));
}

__global__ __launch_bounds__(256) void ln_kernel(const __hip_bfloat16* __restrict__ x, __hip_bfloat16* __restrict__ xn) {
    const int lane = threadIdx.x & 63, row = blockIdx.x * 4 + (threadIdx.x >> 6);
    const u32x4* src = (const u32x4*)(x + (size_t)row * D_);
    u32x4 v0 = src[lane], v1 = src[64 + lane];
    float f[16];
#pragma unroll
    for (int i = 0; i < 4; ++i) { f[2 * i] = bf_lo(v0[i]); f[2 * i + 1] = bf_hi(v0[i]); f[8 + 2 * i] = bf_lo(v1[i]); f[9 + 2 * i] = bf_hi(v1[i]); }
    float s = 0.f, s1 = 0.f;
#pragma unroll
    for (int i = 0; i < 8; ++i) { s += f[i]; s1 += f[8 + i]; }
    const float mean = wave_sum(s + s1) * (1.0f / D_);
    float q = 0.f, q1 = 0.f;
#pragma unroll
    for (int i = 0; i < 8; ++i) { const float d = f[i] - mean, e = f[8 + i] - mean; q += d * d; q1 += e * e; }
    q = wave_sum(q + q1);
    const float mb = rbf(mean);
    const float ri = 1.0f / rbf(sqrtf(rbf(rbf(q * (1.0f / D_)) + 1e-5f)));
    u32x4 o0, o1;
#pragma unroll
    for (int i = 0; i < 4; ++i) {
        const unsigned d0 = pack_bf16(f[2 * i] - mb, f[2 * i + 1] - mb), d1 = pack_bf16(f[8 + 2 * i] - mb, f[9 + 2 * i] - mb);
        o0[i] = pack_bf16(bf_lo(d0) * ri, bf_hi(d0) * ri);
        o1[i] = pack_bf16(bf_lo(d1) * ri, bf_hi(d1) * ri);
    }
    u32x4* dst = (u32x4*)(xn + (size_t)row * D_);
    st16(dst + lane, o0); st16(dst + 64 + lane, o1);
}

constexpr int G1_ABUF = 34816, G1_BOFF = 3 * G1_ABUF, G1_BBUF = 14336, G1_PFOFF = G1_BOFF + 4 * G1_BBUF, G1_LDS = G1_PFOFF + 2048;

template <bool QKV>
__device__ __forceinline__ void gemm1_body(LAS unsigned char* lds, const __hip_bfloat16* __restrict__ xn,
                                           const __hip_bfloat16* __restrict__ w, __hip_bfloat16* __restrict__ act,
                                           const char* __restrict__ pf0, const char* __restrict__ pf1, u32x4* __restrict__ xb,
                                           const int mt, const int ct) {
    constexpr int LDB = QKV ? 3 * D_ : DFF;
    const int tid = threadIdx.x, wid = __builtin_amdgcn_readfirstlane(tid >> 6), lane = tid & 63;
    const int G = wid >> 2, wq = wid & 3, fr = lane & 15, fq = lane >> 4;
    const int m0 = mt * 256;
    int cb0, cstep, cex;
    if (QKV) { cb0 = ct * 64; cstep = D_; cex = 0; }
    else { const int n0 = ct * 224; cb0 = n0 + ((ct & 1) ? 32 : 0); cstep = 64; cex = (ct & 1) ? n0 : n0 + 192; }

    const unsigned lds0 = (unsigned)(uintptr_t)lds;
    const unsigned voffA = (unsigned)((8 * wid + (lane >> 3)) * (D_ * 2) + (((lane & 7) ^ ((4 * wid + (lane >> 4)) & 7)) << 4));
    const unsigned voffB = (unsigned)((8 * wq + (lane >> 3)) * (LDB * 2) + (((lane & 7) ^ ((((lane >> 4) & 1) + 2 * (wq & 1)) << 1)) << 4));
    unsigned voff2 = voffB;
    if (G) {
        if (QKV) voff2 = (unsigned)((4 * wq + ((lane >> 3) & 3)) * (D_ * 2) + (((lane & 7) ^ ((2 * wq + ((lane >> 4) & 1)) & 7)) << 4));
        else voff2 = (unsigned)((8 * wq + ((lane >> 2) & 7)) * (LDB * 2) + ((((lane >> 1) & 1) ^ (wq & 1)) << 5) + ((lane & 1) << 4));
    }
    const char* gA = (const char*)xn + (size_t)m0 * (D_ * 2);
    const char* gB1 = (const char*)w + (size_t)(cb0 + G * cstep) * 2;
    const char* gB2 = G ? (QKV ? (const char*)xn + (size_t)(m0 >= 16 ? m0 - 16 : 0) * (D_ * 2) : (const char*)w + (size_t)cex * 2)
                        : (const char*)w + (size_t)(cb0 + 2 * cstep) * 2;
    constexpr size_t KSB = (size_t)32 * LDB * 2;
    const unsigned dB1 = lds0 + G1_BOFF + G * 4096 + wq * 1024;
    const unsigned dB2 = G ? (QKV ? lds0 + 32768 + wq * 512 : lds0 + G1_BOFF + 12288 + wq * 512) : lds0 + G1_BOFF + 8192 + wq * 1024;
    const bool halo = QKV && G;
    const int lim2 = G ? 32 : 64;
#define STAGE_A(t, kh, ab) do { \
        glds_s(gA + (size_t)(t) * 128 + (size_t)(2 * (kh)) * (64 * D_ * 2), voffA, lds0 + (ab) + (kh) * 16384 + wid * 1024); \
        glds_s(gA + (size_t)(t) * 128 + (size_t)(2 * (kh) + 1) * (64 * D_ * 2), voffA, lds0 + (ab) + (kh) * 16384 + 8192 + wid * 1024); } while (0)
#define STAGE_B(h, s, t, ab) do { \
        glds_s(gB1 + (size_t)(h) * KSB, voffB, dB1 + (s) * G1_BBUF); \
        if (lane < lim2) glds_s(gB2 + (halo ? (size_t)(t) * 128 : (size_t)(h) * KSB), voff2, dB2 + (halo ? (unsigned)(ab) : (unsigned)((s) * G1_BBUF))); } while (0)

    const int q4 = (lane & 15) >> 2, p4 = lane & 3;
    const int aswz = (fq ^ ((fr >> 1) & 7)) << 4;
    const int oA = (wq * 64 + fr) * 128 + aswz, oH = 32768 + fr * 128 + aswz;
    const int bsw = ((q4 >> 1) & 1) + 2 * (fq & 1);
    const int bbase = G1_BOFF + fq * 1024 + q4 * 128 + p4 * 8;
    LAS unsigned char* pB[2] = {lds + bbase + (((2 * G) ^ bsw) << 5), lds + bbase + (((2 * G + 1) ^ bsw) << 5)};
    LAS unsigned char* pEB = lds + G1_BOFF + 12288 + fq * 512 + q4 * 64 + ((G ^ (fq & 1)) << 5) + p4 * 8;

    f32x4 acc[4][6], accE[4];
#pragma unroll
    for (int i = 0; i < 4; ++i) {
        accE[i] = (f32x4){0.f, 0.f, 0.f, 0.f};
#pragma unroll
        for (int c = 0; c < 6; ++c) acc[i][c] = (f32x4){0.f, 0.f, 0.f, 0.f};
    }
    bf16x8 Af[4], Bf[6], Ex;

#define LOADF(s, kh, ab) do { \
        _Pragma("unroll") for (int _c = 0; _c < 6; ++_c) Bf[_c] = lds_tr(pB[_c & 1] + (s) * G1_BBUF + (_c >> 1) * 4096, 512); \
        if (QKV) Ex = lds_b128(lds + (ab) + (oH ^ ((kh) << 6))); else Ex = lds_tr(pEB + (s) * G1_BBUF, 256); \
        _Pragma("unroll") for (int _i = 0; _i < 4; ++_i) Af[_i] = lds_b128(lds + (ab) + (oA ^ ((kh) << 6)) + _i * 2048); \
        SCHED; } while (0)
#define MMA() MMA_(true)
#define MMA_(closing) do { WAIT_L(0); BAR; __builtin_amdgcn_s_setprio(1); \
        _Pragma("unroll") for (int _c = 0; _c < 6; ++_c) _Pragma("unroll") for (int _i = 0; _i < 4; ++_i) \
            acc[_i][_c] = (QKV && _c >= 4) ? mfma16(Af[_i], Bf[_c], acc[_i][_c]) : mfma16(Bf[_c], Af[_i], acc[_i][_c]); \
        if (QKV) { if (wq == 0) { accE[0] = mfma16(Bf[2], Ex, accE[0]); accE[1] = mfma16(Bf[3], Ex, accE[1]); \
                                  accE[2] = mfma16(Ex, Bf[4], accE[2]); accE[3] = mfma16(Ex, Bf[5], accE[3]); } } \
        else { _Pragma("unroll") for (int _i = 0; _i < 4; ++_i) accE[_i] = mfma16(Ex, Af[_i], accE[_i]); } \
        asm volatile("" : "+v"(accE[0]), "+v"(accE[1]), "+v"(accE[2]), "+v"(accE[3])); \
        __builtin_amdgcn_s_setprio(0); if (closing) BAR; SCHED; } while (0)

    STAMP(0, 0);
    STAGE_A(0, 0, 0); STAGE_A(0, 1, 0); STAGE_B(0, 0, 0, 0);
    STAGE_A(1, 0, G1_ABUF); STAGE_B(1, 1, 1, G1_ABUF);
    STAGE_A(1, 1, G1_ABUF); STAGE_B(2, 2, 1, G1_ABUF);
    WAIT_V(8); BAR;
    if (G == 1) BAR;
    STAMP(0, 1);
    int a0 = 0, a1 = G1_ABUF, a2 = 2 * G1_ABUF;
    for (int t = 0; t < 14; t += 2) {
        const int h = 2 * t;
        LOADF(0, 0, a0); STAGE_A(t + 2, 0, a2); STAGE_B(h + 3, 3, t + 2, a2); WAIT_V(8); MMA();
        LOADF(1, 1, a0); STAGE_A(t + 2, 1, a2); STAGE_B(h + 4, 0, t + 2, a2); WAIT_V(8); MMA();
        LOADF(2, 0, a1); STAGE_A(t + 3, 0, a0); STAGE_B(h + 5, 1, t + 3, a0); WAIT_V(8); MMA();
        LOADF(3, 1, a1); STAGE_A(t + 3, 1, a0); STAGE_B(h + 6, 2, t + 3, a0); WAIT_V(8); MMA();
        const int a = a0; a0 = a2; a2 = a1; a1 = a;
    }
    LOADF(0, 0, a0); STAGE_B(31, 3, 15, a1); WAIT_V(6); MMA();
    LOADF(1, 1, a0);
    glds4_s(pf0, tid * 64, lds0 + G1_PFOFF + wid * 256); glds4_s(pf1, lane * 64, lds0 + G1_PFOFF + wid * 256);
    st16(xb, (u32x4){XB_EMPTY, XB_EMPTY, XB_EMPTY, XB_EMPTY}); st16(xb + 512, (u32x4){XB_EMPTY, XB_EMPTY, XB_EMPTY, XB_EMPTY});
    WAIT_V(6); MMA();
    LOADF(2, 0, a1); WAIT_V(4); MMA();
    LOADF(3, 1, a1); MMA_(G == 0);
    STAMP(0, 2);
#undef STAGE_A
#undef STAGE_B
#undef LOADF
#undef MMA
#undef MMA_
    const int rt0 = mt * 16 + wq * 4;
    char* gO = (char*)act + lane * 16;
    if (!QKV) {
#pragma unroll
        for (int i = 0; i < 4; ++i) {
#pragma unroll
            for (int j = 0; j < 3; ++j) {
                const f32x4 a = acc[i][2 * j], b = acc[i][2 * j + 1];
                const u32x4 o = {relu2(pack_bf16(a[0], a[1])), relu2(pack_bf16(a[2], a[3])), relu2(pack_bf16(b[0], b[1])), relu2(pack_bf16(b[2], b[3]))};
                st16(gO + act_block(rt0 + i, ((D_ + cb0) >> 5) + 2 * j + G), o);
            }
            const f32x4 e = accE[i];
            const u32x2 o2 = {relu2(pack_bf16(e[0], e[1])), relu2(pack_bf16(e[2], e[3]))};
            st8(gO + act_block(rt0 + i, (D_ + cex) >> 5) + G * 8, o2);
        }
    } else {
        u32x4 Qf[4], Kf[5], Vf[5];
#pragma unroll
        for (int i = 0; i < 4; ++i) {
            Qf[i] = pack8(acc[i][0], acc[i][1]); Kf[i + 1] = pack8(acc[i][2], acc[i][3]); Vf[i + 1] = pack8(acc[i][4], acc[i][5]);
        }
        LAS u32x4* ex = (LAS u32x4*)(lds + wid * 2048) + lane;
        ex[0] = Kf[4]; ex[64] = Vf[4];
        const int par = fr & 1;
        const bool cura = fr - 4 * fq - par >= 0, curb = fr - 4 * fq - par - 2 >= 0;
        const f32x4 z = {0.f, 0.f, 0.f, 0.f};
        auto half = [](unsigned a, unsigned b) { return __builtin_bit_cast(s16x4, (u32x2){a, b}); };
        auto attend = [&](const int i) {
            const f32x4 s0 = mfma16(__builtin_bit_cast(bf16x8, Kf[i]), __builtin_bit_cast(bf16x8, Qf[i]), z);
            const f32x4 s1 = mfma16(__builtin_bit_cast(bf16x8, Kf[i + 1]), __builtin_bit_cast(bf16x8, Qf[i]), z);
            float la = cura ? (par ? s1[1] : s1[0]) : (par ? s0[1] : s0[0]);
            float lb = curb ? (par ? s1[3] : s1[2]) : (par ? s0[3] : s0[2]);
            const unsigned l1 = pack_bf16(la, lb);
            const unsigned l2 = pack_bf16(bf_lo(l1) * 0.17677669529663687f, bf_hi(l1) * 0.17677669529663687f);
            la = bf_lo(l2); lb = bf_hi(l2);
            if (i == 0 && rt0 == 0) { la = cura ? la : -1e9f; lb = curb ? lb : -1e9f; }
            const float mx = xred4<true>(vmax(la, lb));
            const float ea = __expf(la - mx), eb = __expf(lb - mx);
            const float inv = __builtin_amdgcn_rcpf(xred4<false>(ea + eb));
            const unsigned pw = pack_bf16(ea * inv, eb * inv);
            const unsigned wa = (pw & 0xffffu) << (16 * par), wb = (pw >> 16) << (16 * par);
            const s16x4 P0 = half(cura ? 0u : wa, curb ? 0u : wb), P1 = half(cura ? wa : 0u, curb ? wb : 0u);
            f32x4 o0 = __builtin_amdgcn_mfma_f32_16x16x16bf16_1k(half(Vf[i][0], Vf[i][1]), P0, z, 0, 0, 0);
            f32x4 o1 = __builtin_amdgcn_mfma_f32_16x16x16bf16_1k(half(Vf[i][2], Vf[i][3]), P0, z, 0, 0, 0);
            o0 = __builtin_amdgcn_mfma_f32_16x16x16bf16_1k(half(Vf[i + 1][0], Vf[i + 1][1]), P1, o0, 0, 0, 0);
            o1 = __builtin_amdgcn_mfma_f32_16x16x16bf16_1k(half(Vf[i + 1][2], Vf[i + 1][3]), P1, o1, 0, 0, 0);
            st16(gO + act_block(rt0 + i, ct * 2 + G), pack8(o0, o1));
        };
        attend(3); attend(2); attend(1);
        WAIT_L(0); BAR;
        STAMP(0, 3);
        if (wq == 0) { Kf[0] = pack8(accE[0], accE[1]); Vf[0] = pack8(accE[2], accE[3]); }
        else { Kf[0] = ex[-128]; Vf[0] = ex[-64]; }
        attend(0);
    }
    STAMP(0, 4);
    STAMP(0, 5);
}

__global__ __launch_bounds__(512, 2) void gemm1_kernel(const __hip_bfloat16* __restrict__ xn, const __hip_bfloat16* __restrict__ wqkv,
                                                       const __hip_bfloat16* __restrict__ w1, __hip_bfloat16* __restrict__ act,
                                                       const __hip_bfloat16* __restrict__ wo, const __hip_bfloat16* __restrict__ w2,
                                                       unsigned* __restrict__ xbuf) {
    extern __shared__ __attribute__((aligned(16))) unsigned char smem[];
    LAS unsigned char* lds = (LAS unsigned char*)smem;
    const int xcd = blockIdx.x & 7, j = blockIdx.x >> 3;
    const int ct = xcd * 2 + (j & 1), mt = j >> 2;
    const int wid = __builtin_amdgcn_readfirstlane(threadIdx.x >> 6);
    const char* pf0 = blockIdx.x < 224 ? (const char*)w2 + (size_t)blockIdx.x * 32768 : (const char*)wo + (size_t)(blockIdx.x - 224) * 32768;
    const char* pf1 = (const char*)wo + (1u << 20) + (size_t)((blockIdx.x * 8 + wid) & 255) * 4096;
    u32x4* xb = (u32x4*)xbuf + blockIdx.x * 1024 + threadIdx.x;
    if ((j >> 1) & 1) gemm1_body<true>(lds, xn, wqkv, act, pf0, pf1, xb, mt, ct);
    else gemm1_body<false>(lds, xn, w1, act, pf0, pf1, xb, mt, ct);
}

#define PROBE_GAP 4
constexpr int G2_NS = 5, G2_STAGE = 32768, G2_LDS = G2_NS * G2_STAGE, G2_NT = KACT / 64 / 2;
constexpr int G2_XOFF = (G2_NT % G2_NS) * G2_STAGE, G2_ROFF = 65536;
static_assert(G2_XOFF >= 32768 && G2_XOFF + 16384 <= G2_ROFF, "LDS map: probes at 0 and 128 KiB, 32 KiB each");

__global__ __launch_bounds__(768, 3) void gemm2_kernel(const __hip_bfloat16* __restrict__ act, const __hip_bfloat16* __restrict__ wo,
                                                       const __hip_bfloat16* __restrict__ w2, const __hip_bfloat16* __restrict__ x,
                                                       __hip_bfloat16* __restrict__ out, unsigned* __restrict__ xbuf) {
    extern __shared__ __attribute__((aligned(16))) unsigned char smem[];
    LAS unsigned char* lds = (LAS unsigned char*)smem;
    const int tid = threadIdx.x, wid = __builtin_amdgcn_readfirstlane(tid >> 6), lane = tid & 63;
    const int G = wid >> 2, wq = wid & 3, wm = wq >> 1, wn = wq & 1, fr = lane & 15, fq = lane >> 4;
    const int xcd = blockIdx.x & 7, jb = blockIdx.x >> 3;
    const int sp = xcd & 1, mt = (xcd >> 1) * 4 + (jb & 3), nt = jb >> 2;
    const int m0 = mt * 128, n0 = nt * 128, kt0 = sp * G2_NT;
    const int pair = (mt * 8 + nt) * 2;

    if (G == 2) {
        const unsigned voffA = (unsigned)(wq * 1024 + lane * 16);
        const unsigned voffB = (unsigned)((8 * wq + (lane >> 3)) * (D_ * 2) + (((lane & 7) ^ ((lane >> 4) << 1)) << 4));
        const char* gA = (const char*)act + act_block(mt * 8, kt0 * 2);
        const unsigned ldsw = (unsigned)(uintptr_t)lds + wq * 1024;
        auto stage = [&](int t, int slot) {
            const int kt = kt0 + t;
            const char* gB = (kt < 16 ? (const char*)wo + (size_t)kt * (64 * D_ * 2) : (const char*)w2 + (size_t)(kt - 16) * (64 * D_ * 2)) + n0 * 2;
            const unsigned d = ldsw + slot * G2_STAGE;
#pragma unroll
            for (int u = 0; u < 4; ++u) glds_s(gB + (size_t)(u & 1) * (32 * D_ * 2) + (u >> 1) * 128, voffB, d + 16384 + (u >> 1) * 8192 + (u & 1) * 4096);
#pragma unroll
            for (int u = 0; u < 4; ++u) glds_s(gA + (size_t)t * (128 * 2048) + u * 4096, voffA, d + u * 4096);
        };
        for (int t = 0; t < 4; ++t) stage(t, t);
        WAIT_V(24); BAR;
        int ss = 4;
        for (int s = 0; s < G2_NT - 4; ++s) {
            stage(s + 4, ss);
            ss = ss == G2_NS - 1 ? 0 : ss + 1;
            WAIT_V(24); BAR;
        }
        const char* gX = (const char*)x + (size_t)(m0 + (wq >> 1) * 64 + (2 * (wq & 1) + sp) * 16) * (D_ * 2) + n0 * 2;
        const unsigned voffX = (unsigned)((lane >> 4) * (D_ * 2) + (((lane & 15) ^ (lane >> 4)) << 4));
#pragma unroll
        for (int u = 0; u < 4; ++u) glds_s(gX + u * (4 * D_ * 2), voffX ^ (u << 6), (unsigned)(uintptr_t)lds + G2_XOFF + wq * 4096 + u * 1024);
        WAIT_V(20); BAR;
        WAIT_V(12); BAR;
        WAIT_V(4); BAR;
        BAR; BAR;
        WAIT_V(0); BAR;
        return;
    }

    const int q4 = (lane & 15) >> 2, p4 = lane & 3;
    const int aoff[2] = {wm * 8192 + lane * 16 + sp * 2048, wm * 8192 + lane * 16 - sp * 2048};
    const int boff = 16384 + wn * 8192 + fq * 512 + q4 * 128 + ((((q4 >> 1) & 1) + 2 * (fq & 1)) << 5) + p4 * 8;
    STAMP(1, 0);
    f32x4 acc[4][4];
#pragma unroll
    for (int i = 0; i < 4; ++i)
#pragma unroll
        for (int c = 0; c < 4; ++c) acc[i][c] = (f32x4){0.f, 0.f, 0.f, 0.f};
    bf16x8 Af[4][2], Bf[4][2];

    BAR;
    if (G == 1) BAR;
    STAMP(1, 1);
    int slot = G;
    auto step = [&]() {
        LAS unsigned char* sb = lds + slot * G2_STAGE;
#pragma unroll
        for (int k = 0; k < 2; ++k) {
#pragma unroll
            for (int c = 0; c < 4; ++c) Bf[c][k] = lds_tr(sb + (boff ^ (c << 5)) + k * 4096, 2048);
#pragma unroll
            for (int i = 0; i < 4; ++i) Af[i][k] = lds_b128(sb + aoff[i & 1] + i * 2048 + k * 1024);
        }
        WAIT_L(0); BAR;
        __builtin_amdgcn_s_setprio(1);
#pragma unroll
        for (int k = 0; k < 2; ++k)
#pragma unroll
            for (int c = 0; c < 4; ++c)
#pragma unroll
                for (int i = 0; i < 4; ++i) acc[i][c] = mfma16(Bf[c][k], Af[i][k], acc[i][c]);
        __builtin_amdgcn_s_setprio(0);
        BAR; SCHED;
        slot += 2; if (slot >= G2_NS) slot -= G2_NS;
    };
#pragma unroll 1
    for (int t = 0; t < G2_NT / 2; ++t) step();
    STAMP(1, 2);
    LAS f32x4* r1 = (LAS f32x4*)(lds + G2_ROFF + wq * 8192) + lane;
    LAS f32x4* r2 = r1 + 2048;
    const int xo = wq * 4096 + lane * 16;
    if (G == 1) {
        const __amdgpu_buffer_rsrc_t rs = __builtin_amdgcn_make_buffer_rsrc((void*)xbuf, 0, 256 * 16384, 0x00020000);
        f32x4 t[8];
#pragma unroll
        for (int e = 0; e < 8; ++e) t[e] = r1[e * 64];
        SCHED;
#pragma unroll
        for (int e = 0; e < 4; ++e)
            __builtin_amdgcn_raw_buffer_store_b128(pack8(acc[(e & 2) + 1][2 * (e & 1)] + t[2 * e], acc[(e & 2) + 1][2 * (e & 1) + 1] + t[2 * e + 1]),
                                                   rs, (pair + sp) * 16384 + xo + e * 1024, 0, 16);
#pragma unroll
        for (int u = 0; u < 2; ++u)
#pragma unroll
            for (int c = 0; c < 4; ++c) r2[(u * 4 + c) * 64] = acc[2 * u][c];
        WAIT_L(0); BAR;
        STAMP(1, 3);
        return;
    }
#pragma unroll
    for (int u = 0; u < 2; ++u)
#pragma unroll
        for (int c = 0; c < 4; ++c) r1[(u * 4 + c) * 64] = acc[2 * u + 1][c];
    WAIT_L(0); BAR;
#define PIN8 asm volatile("" : "+v"(acc[0][0]), "+v"(acc[0][1]), "+v"(acc[0][2]), "+v"(acc[0][3]), "+v"(acc[2][0]), "+v"(acc[2][1]), "+v"(acc[2][2]), "+v"(acc[2][3]))
    BAR;
    STAMP(1, 3);
    u32x4 rr[4];
    {
        const char* gx = (const char*)xbuf + (size_t)(pair + (sp ^ 1)) * 16384;
        auto probe = [&](int k) {
            const unsigned d = (unsigned)(uintptr_t)lds + (k >> 1) * 131072 + wq * 8192 + (k & 1) * 4096;
#pragma unroll
            for (int e = 0; e < 4; ++e)
                asm volatile("s_mov_b32 m0, %2\n\ts_nop 0\n\tglobal_load_lds_dwordx4 %0, %1 sc1" ::"v"(xo), "s"(gx + e * 1024), "s"(d + e * 1024) : "memory");
        };
        probe(0);
#pragma unroll
        for (int u = 0; u < 2; ++u)
#pragma unroll
            for (int c = 0; c < 4; ++c) {
                const u32x2 xr = *(LAS u32x2*)(lds + G2_XOFF + (wm * 2 + u) * 4096 + fr * 256 + (((wn * 8 + c * 2 + (fq >> 1)) ^ fr) << 4) + (fq & 1) * 8);
                acc[2 * u][c] += r2[(u * 4 + c) * 64] + (f32x4){bf_lo(xr[0]), bf_hi(xr[0]), bf_lo(xr[1]), bf_hi(xr[1])};
            }
        PIN8;
#pragma unroll
        for (int k = 1; k < 4; ++k) { probe(k); __builtin_amdgcn_s_sleep(PROBE_GAP); }
        for (int spin = 0; spin < (1 << 16); ++spin) {
            const int k = spin & 3;
            WAIT_V(12);
            LAS u32x4* p = (LAS u32x4*)(lds + (k >> 1) * 131072 + wq * 8192 + (k & 1) * 4096) + lane;
            unsigned all = 0u;
#pragma unroll
            for (int e = 0; e < 4; ++e) {
                rr[e] = p[e * 64];
                all = max(max(max(all, rr[e][0]), max(rr[e][1], rr[e][2])), rr[e][3]);
            }
            if (__builtin_amdgcn_ballot_w64(all == XB_EMPTY) == 0) break;
            probe(k);
        }
    }
    STAMP(1, 4);
#pragma unroll
    for (int u = 0; u < 2; ++u) {
        char* gO = (char*)out + (size_t)(m0 + wm * 64 + (2 * u + sp) * 16 + fr) * (D_ * 2) + (n0 + wn * 64 + (fq & 1) * 16 + (fq >> 1) * 8) * 2;
#pragma unroll
        for (int j = 0; j < 2; ++j) {
            const u32x4 r = rr[u * 2 + j];
            const f32x4 a = acc[2 * u][2 * j], b = acc[2 * u][2 * j + 1];
            const auto rx = __builtin_amdgcn_permlane16_swap(pack_bf16(a[0] + bf_lo(r[0]), a[1] + bf_hi(r[0])), pack_bf16(b[0] + bf_lo(r[2]), b[1] + bf_hi(r[2])), false, false);
            const auto ry = __builtin_amdgcn_permlane16_swap(pack_bf16(a[2] + bf_lo(r[1]), a[3] + bf_hi(r[1])), pack_bf16(b[2] + bf_lo(r[3]), b[3] + bf_hi(r[3])), false, false);
            st16(gO + j * 64, (u32x4){rx[0], ry[0], rx[1], ry[1]});
        }
    }
    STAMP(1, 5);
}

extern "C" void kernel_launch(void* const* d_in, const int* in_sizes, int n_in, void* d_out, int out_size, void* d_ws,
                              size_t ws_size, hipStream_t stream) {
    const __hip_bfloat16* x = (const __hip_bfloat16*)d_in[0];
    const __hip_bfloat16* wqkv = (const __hip_bfloat16*)d_in[1];
    const __hip_bfloat16* wo = (const __hip_bfloat16*)d_in[2];
    const __hip_bfloat16* w1 = (const __hip_bfloat16*)d_in[4];
    const __hip_bfloat16* w2 = (const __hip_bfloat16*)d_in[5];
    char* ws = (char*)d_ws;
    __hip_bfloat16* xn = (__hip_bfloat16*)ws;
    __hip_bfloat16* act = (__hip_bfloat16*)(ws + (4u << 20));
    unsigned* xbuf = (unsigned*)(ws + (24u << 20));
    (void)hipFuncSetAttribute((const void*)gemm1_kernel, hipFuncAttributeMaxDynamicSharedMemorySize, G1_LDS);
    (void)hipFuncSetAttribute((const void*)gemm2_kernel, hipFuncAttributeMaxDynamicSharedMemorySize, G2_LDS);
    ln_kernel<<<S_ / 4, 256, 0, stream>>>(x, xn);
    gemm1_kernel<<<256, 512, G1_LDS, stream>>>(xn, wqkv, w1, act, wo, w2, xbuf);
    gemm2_kernel<<<256, 768, G2_LDS, stream>>>(act, wo, w2, x, (__hip_bfloat16*)d_out, xbuf);
}
```

```cpp
#include <hip/hip_runtime.h>
#include <hip/hip_bf16.h>
#include <stdint.h>

#define LAS __attribute__((address_space(3)))
#define GAS __attribute__((address_space(1)))
typedef __attribute__((ext_vector_type(8))) short bf16x8;
typedef __attribute__((ext_vector_type(4))) short s16x4;
typedef __attribute__((ext_vector_type(4))) float f32x4;
typedef __attribute__((ext_vector_type(2))) float f32x2;
typedef __attribute__((ext_vector_type(2))) __bf16 bf16x2_t;
typedef __attribute__((ext_vector_type(4))) unsigned u32x4;
typedef __attribute__((ext_vector_type(2))) unsigned u32x2;

constexpr int S_ = 2048, D_ = 1024, DFF = 3584, KACT = D_ + DFF;
__device__ __forceinline__ size_t act_block(int rt, int kb) { return (size_t)((kb >> 1) * 128 + rt) * 2048 + (size_t)(kb & 1) * 1024; }

constexpr unsigned XB_EMPTY = 0xffffffffu;

#define WAIT_V(n) asm volatile("s_waitcnt vmcnt(" #n ")" ::: "memory")
#define WAIT_L(n) asm volatile("s_waitcnt lgkmcnt(" #n ")" ::: "memory")
#define BAR do { __builtin_amdgcn_s_barrier(); asm volatile("" ::: "memory"); } while (0)
#define SCHED __builtin_amdgcn_sched_barrier(0)
#define STAMP(k, i)
#define GLDS(src, dst) __builtin_amdgcn_global_load_lds((const unsigned*)(src), (LAS unsigned*)(dst), 16, 0, 0)

__device__ __forceinline__ void glds_s(const void* sbase, unsigned voff, unsigned ldsaddr) {
    asm volatile("s_mov_b32 m0, %2\n\ts_nop 0\n\tglobal_load_lds_dwordx4 %0, %1" ::"v"(voff), "s"(sbase), "s"(ldsaddr) : "memory");
}
template <int IMM>
__device__ __forceinline__ void glds_i(const void* sbase, unsigned voff, unsigned la) {
    asm volatile("s_add_i32 m0, %2, %3\n\ts_nop 0\n\tglobal_load_lds_dwordx4 %0, %1" ::"v"(voff), "s"(sbase), "s"(la), "n"(IMM) : "memory", "scc");
}
__device__ __forceinline__ void glds_a(const void* sbase, unsigned voff, unsigned la, unsigned lb) {
    asm volatile("s_add_i32 m0, %2, %3\n\ts_nop 0\n\tglobal_load_lds_dwordx4 %0, %1" ::"v"(voff), "s"(sbase), "s"(la), "s"(lb) : "memory", "scc");
}
__device__ __forceinline__ void glds4_s(const void* sbase, unsigned voff, unsigned ldsaddr) {
    asm volatile("s_mov_b32 m0, %2\n\ts_nop 0\n\tglobal_load_lds_dword %0, %1" ::"v"(voff), "s"(sbase), "s"(ldsaddr) : "memory");
}
__device__ __forceinline__ unsigned pack_bf16(float a, float b) {
    f32x2 v = {a, b};
    bf16x2_t r = __builtin_convertvector(v, bf16x2_t);
    return __builtin_bit_cast(unsigned, r);
}
__device__ __forceinline__ float bf_lo(unsigned u) { return __builtin_bit_cast(float, u << 16); }
__device__ __forceinline__ float bf_hi(unsigned u) { return __builtin_bit_cast(float, u & 0xffff0000u); }
__device__ __forceinline__ float rbf(float a) { return bf_lo(pack_bf16(a, a)); }
__device__ __forceinline__ bf16x8 lds_b128(LAS unsigned char* p) { return *(LAS bf16x8*)p; }
__device__ __forceinline__ bf16x8 lds_tr(LAS unsigned char* p, int hi_off) {
    s16x4 lo = __builtin_amdgcn_ds_read_tr16_b64_v4i16((LAS s16x4*)p);
    s16x4 hi = __builtin_amdgcn_ds_read_tr16_b64_v4i16((LAS s16x4*)(p + hi_off));
    return __builtin_shufflevector(lo, hi, 0, 1, 2, 3, 4, 5, 6, 7);
}
__device__ __forceinline__ f32x4 mfma16(bf16x8 a, bf16x8 b, f32x4 c) {
    return __builtin_amdgcn_mfma_f32_16x16x32_bf16(a, b, c, 0, 0, 0);
}
__device__ __forceinline__ f32x4 sel4(bool c, f32x4 a, f32x4 b) {
    f32x4 r;
#pragma unroll
    for (int e = 0; e < 4; ++e) r[e] = c ? a[e] : b[e];
    return r;
}
__device__ __forceinline__ u32x4 pack8(f32x4 a, f32x4 b) {
    return (u32x4){pack_bf16(a[0], a[1]), pack_bf16(a[2], a[3]), pack_bf16(b[0], b[1]), pack_bf16(b[2], b[3])};
}
__device__ __forceinline__ unsigned relu2(unsigned u) {
    typedef __attribute__((ext_vector_type(2))) short s16x2;
    return __builtin_bit_cast(unsigned, __builtin_elementwise_max(__builtin_bit_cast(s16x2, u), (s16x2){0, 0}));
}
#define ST_MODE 3
#define ST_POL " sc0 sc1"
__device__ __forceinline__ void st16(void* p, u32x4 v) { asm volatile("global_store_dwordx4 %0, %1, off" ST_POL "\n\ts_nop 1" ::"v"(p), "v"(v) : "memory"); }
__device__ __forceinline__ float u2f(unsigned u) { return __builtin_bit_cast(float, u); }
__device__ __forceinline__ unsigned f2u(float f) { return __builtin_bit_cast(unsigned, f); }
__device__ __forceinline__ float vmax(float a, float b) { float r; asm("v_max_f32 %0, %1, %2" : "=v"(r) : "v"(a), "v"(b)); return r; }
template <bool MAX>
__device__ __forceinline__ float xred4(float v) {
    const auto a = __builtin_amdgcn_permlane16_swap(f2u(v), f2u(v), false, false);
    v = MAX ? vmax(u2f(a[0]), u2f(a[1])) : u2f(a[0]) + u2f(a[1]);
    const auto b = __builtin_amdgcn_permlane32_swap(f2u(v), f2u(v), false, false);
    return MAX ? vmax(u2f(b[0]), u2f(b[1])) : u2f(b[0]) + u2f(b[1]);
}

template <int CTRL>
__device__ __forceinline__ float dpp_add(float v) { return v + u2f(__builtin_amdgcn_update_dpp(0u, f2u(v), CTRL, 0xf, 0xf, true)); }
__device__ __forceinline__ float wave_sum(float v) {
    return xred4<false>(dpp_add<0x140>(dpp_add<0x141>(dpp_add<0x4e>(dpp_add<0xb1>(v)))));
}

__global__ __launch_bounds__(256) void ln_kernel(const __hip_bfloat16* __restrict__ x, __hip_bfloat16* __restrict__ xn) {
    const int lane = threadIdx.x & 63, row = blockIdx.x * 4 + (threadIdx.x >> 6);
    const u32x4* src = (const u32x4*)(x + (size_t)row * D_);
    u32x4 v0 = src[lane], v1 = src[64 + lane];
    float f[16];
#pragma unroll
    for (int i = 0; i < 4; ++i) { f[2 * i] = bf_lo(v0[i]); f[2 * i + 1] = bf_hi(v0[i]); f[8 + 2 * i] = bf_lo(v1[i]); f[9 + 2 * i] = bf_hi(v1[i]); }
    float s = 0.f, s1 = 0.f;
#pragma unroll
    for (int i = 0; i < 8; ++i) { s += f[i]; s1 += f[8 + i]; }
    const float mean = wave_sum(s + s1) * (1.0f / D_);
    float q = 0.f, q1 = 0.f;
#pragma unroll
    for (int i = 0; i < 8; ++i) { const float d = f[i] - mean, e = f[8 + i] - mean; q += d * d; q1 += e * e; }
    q = wave_sum(q + q1);
    const float mb = rbf(mean);
    const float ri = 1.0f / rbf(sqrtf(rbf(rbf(q * (1.0f / D_)) + 1e-5f)));
    u32x4 o0, o1;
#pragma unroll
    for (int i = 0; i < 4; ++i) {
        const unsigned d0 = pack_bf16(f[2 * i] - mb, f[2 * i + 1] - mb), d1 = pack_bf16(f[8 + 2 * i] - mb, f[9 + 2 * i] - mb);
        o0[i] = pack_bf16(bf_lo(d0) * ri, bf_hi(d0) * ri);
        o1[i] = pack_bf16(bf_lo(d1) * ri, bf_hi(d1) * ri);
    }
    u32x4* dst = (u32x4*)(xn + (size_t)row * D_);
    st16(dst + lane, o0); st16(dst + 64 + lane, o1);
}

constexpr int G1_ABUF = 34816, G1_BOFF = 3 * G1_ABUF, G1_BBUF = 14336, G1_PFOFF = G1_BOFF + 4 * G1_BBUF, G1_LDS = G1_PFOFF + 2048;

template <bool QKV>
__device__ __forceinline__ void gemm1_body(LAS unsigned char* lds, const __hip_bfloat16* __restrict__ xn,
                                           const __hip_bfloat16* __restrict__ w, __hip_bfloat16* __restrict__ act,
                                           const char* __restrict__ pf0, const char* __restrict__ pf1, u32x4* __restrict__ xb,
                                           const int mt, const int ct) {
    constexpr int LDB = QKV ? 3 * D_ : DFF;
    const int tid = threadIdx.x, wid = __builtin_amdgcn_readfirstlane(tid >> 6), lane = tid & 63;
    const int G = wid >> 2, wq = wid & 3, fr = lane & 15, fq = lane >> 4;
    const int m0 = mt * 256;
    int cb0, cstep, cex;
    if (QKV) { cb0 = ct * 64; cstep = D_; cex = 0; }
    else { const int n0 = ct * 224; cb0 = n0 + ((ct & 1) ? 32 : 0); cstep = 64; cex = (ct & 1) ? n0 : n0 + 192; }

    const unsigned lds0 = (unsigned)(uintptr_t)lds;
    const unsigned voffA = (unsigned)((8 * wid + (lane >> 3)) * (D_ * 2) + (((lane & 7) ^ ((4 * wid + (lane >> 4)) & 7)) << 4));
    const unsigned voffB = (unsigned)((8 * wq + (lane >> 3)) * (LDB * 2) + (((lane & 7) ^ ((((lane >> 4) & 1) + 2 * (wq & 1)) << 1)) << 4));
    unsigned voff2 = voffB;
    if (G) {
        if (QKV) voff2 = (unsigned)((4 * wq + ((lane >> 3) & 3)) * (D_ * 2) + (((lane & 7) ^ ((2 * wq + ((lane >> 4) & 1)) & 7)) << 4));
        else voff2 = (unsigned)((8 * wq + ((lane >> 2) & 7)) * (LDB * 2) + ((((lane >> 1) & 1) ^ (wq & 1)) << 5) + ((lane & 1) << 4));
    }
    const char* gA = (const char*)xn + (size_t)m0 * (D_ * 2);
    const char* gB1 = (const char*)w + (size_t)(cb0 + G * cstep) * 2;
    const char* gB2 = G ? (QKV ? (const char*)xn + (size_t)(m0 >= 16 ? m0 - 16 : 0) * (D_ * 2) : (const char*)w + (size_t)cex * 2)
                        : (const char*)w + (size_t)(cb0 + 2 * cstep) * 2;
    constexpr size_t KSB = (size_t)32 * LDB * 2;
    const unsigned dB1 = lds0 + G1_BOFF + G * 4096 + wq * 1024;
    const unsigned dB2 = G ? (QKV ? lds0 + 32768 + wq * 512 : lds0 + G1_BOFF + 12288 + wq * 512) : lds0 + G1_BOFF + 8192 + wq * 1024;
    const bool halo = QKV && G;
    const int lim2 = G ? 32 : 64;
#define STAGE_A(t, kh, ab) do { \
        glds_s(gA + (size_t)(t) * 128 + (size_t)(2 * (kh)) * (64 * D_ * 2), voffA, lds0 + (ab) + (kh) * 16384 + wid * 1024); \
        glds_s(gA + (size_t)(t) * 128 + (size_t)(2 * (kh) + 1) * (64 * D_ * 2), voffA, lds0 + (ab) + (kh) * 16384 + 8192 + wid * 1024); } while (0)
#define STAGE_B(h, s, t, ab) do { \
        glds_s(gB1 + (size_t)(h) * KSB, voffB, dB1 + (s) * G1_BBUF); \
        if (lane < lim2) glds_s(gB2 + (halo ? (size_t)(t) * 128 : (size_t)(h) * KSB), voff2, dB2 + (halo ? (unsigned)(ab) : (unsigned)((s) * G1_BBUF))); } while (0)

    const int q4 = (lane & 15) >> 2, p4 = lane & 3;
    const int aswz = (fq ^ ((fr >> 1) & 7)) << 4;
    const int oA = (wq * 64 + fr) * 128 + aswz, oH = 32768 + fr * 128 + aswz;
    const int bsw = ((q4 >> 1) & 1) + 2 * (fq & 1);
    const int bbase = G1_BOFF + fq * 1024 + q4 * 128 + p4 * 8;
    LAS unsigned char* pB[2] = {lds + bbase + (((2 * G) ^ bsw) << 5), lds + bbase + (((2 * G + 1) ^ bsw) << 5)};
    LAS unsigned char* pEB = lds + G1_BOFF + 12288 + fq * 512 + q4 * 64 + ((G ^ (fq & 1)) << 5) + p4 * 8;

    f32x4 acc[4][6], accE[4];
#pragma unroll
    for (int i = 0; i < 4; ++i) {
        accE[i] = (f32x4){0.f, 0.f, 0.f, 0.f};
#pragma unroll
        for (int c = 0; c < 6; ++c) acc[i][c] = (f32x4){0.f, 0.f, 0.f, 0.f};
    }
    bf16x8 Af[4], Bf[6], Ex;

#define LOADF(s, kh, ab) do { \
        _Pragma("unroll") for (int _c = 0; _c < 6; ++_c) Bf[_c] = lds_tr(pB[_c & 1] + (s) * G1_BBUF + (_c >> 1) * 4096, 512); \
        if (QKV) Ex = lds_b128(lds + (ab) + (oH ^ ((kh) << 6))); else Ex = lds_tr(pEB + (s) * G1_BBUF, 256); \
        _Pragma("unroll") for (int _i = 0; _i < 4; ++_i) Af[_i] = lds_b128(lds + (ab) + (oA ^ ((kh) << 6)) + _i * 2048); \
        SCHED; } while (0)
#define MMA() MMA_(true)
#define MMA_(closing) do { WAIT_L(0); BAR; __builtin_amdgcn_s_setprio(1); \
        _Pragma("unroll") for (int _c = 0; _c < 6; ++_c) _Pragma("unroll") for (int _i = 0; _i < 4; ++_i) \
            acc[_i][_c] = (QKV && _c >= 4) ? mfma16(Af[_i], Bf[_c], acc[_i][_c]) : mfma16(Bf[_c], Af[_i], acc[_i][_c]); \
        if (QKV) { if (wq == 0) { accE[0] = mfma16(Bf[2], Ex, accE[0]); accE[1] = mfma16(Bf[3], Ex, accE[1]); \
                                  accE[2] = mfma16(Ex, Bf[4], accE[2]); accE[3] = mfma16(Ex, Bf[5], accE[3]); } } \
        else { _Pragma("unroll") for (int _i = 0; _i < 4; ++_i) accE[_i] = mfma16(Ex, Af[_i], accE[_i]); } \
        asm volatile("" : "+v"(accE[0]), "+v"(accE[1]), "+v"(accE[2]), "+v"(accE[3])); \
        __builtin_amdgcn_s_setprio(0); if (closing) BAR; SCHED; } while (0)

    STAMP(0, 0);
    STAGE_A(0, 0, 0); STAGE_A(0, 1, 0); STAGE_B(0, 0, 0, 0);
    STAGE_A(1, 0, G1_ABUF); STAGE_B(1, 1, 1, G1_ABUF);
    STAGE_A(1, 1, G1_ABUF); STAGE_B(2, 2, 1, G1_ABUF);
    WAIT_V(8); BAR;
    if (G == 1) BAR;
    STAMP(0, 1);
    int a0 = 0, a1 = G1_ABUF, a2 = 2 * G1_ABUF;
    {
        const char* pA = gA + 256;
        const char* p1 = gB1 + 3 * KSB;
        const char* p2 = gB2 + (halo ? (size_t)256 : 3 * KSB);
        const unsigned inc0 = halo ? 0u : (unsigned)KSB, inc1 = halo ? 128u : (unsigned)KSB, hm = halo ? ~0u : 0u, adw = lds0 + wid * 1024;
        unsigned vA[4], cs[4];
#pragma unroll
        for (int u = 0; u < 4; ++u) { vA[u] = voffA + u * (64 * D_ * 2); cs[u] = dB2 + (halo ? 0u : (unsigned)(u * G1_BBUF)); }
#define STAGE(kh, s, aw, ah) do { \
        glds_i<(kh) * 16384>(pA, vA[2 * (kh)], aw); glds_i<(kh) * 16384 + 8192>(pA, vA[2 * (kh) + 1], aw); glds_i<(s) * G1_BBUF>(p1, voffB, dB1); \
        if (lane < lim2) { if (QKV) glds_a(p2, voff2, ah, cs[s]); else glds_i<(s) * G1_BBUF>(p2, voff2, dB2); } \
        p1 += KSB; p2 += (kh) ? inc1 : inc0; if (kh) pA += 128; \
        asm volatile("" : "+s"(pA), "+s"(p1), "+s"(p2)); } while (0)
        for (int t = 0; t < 14; t += 2) {
            const unsigned w2 = adw + a2, w0 = adw + a0, h2 = a2 & hm, h0 = a0 & hm;
            LOADF(0, 0, a0); STAGE(0, 3, w2, h2); WAIT_V(8); MMA();
            LOADF(1, 1, a0); STAGE(1, 0, w2, h2); WAIT_V(8); MMA();
            LOADF(2, 0, a1); STAGE(0, 1, w0, h0); WAIT_V(8); MMA();
            LOADF(3, 1, a1); STAGE(1, 2, w0, h0); WAIT_V(8); MMA();
            const int a = a0; a0 = a2; a2 = a1; a1 = a;
        }
#undef STAGE
    }
    LOADF(0, 0, a0); STAGE_B(31, 3, 15, a1); WAIT_V(6); MMA();
    LOADF(1, 1, a0);
    glds4_s(pf0, tid * 64, lds0 + G1_PFOFF + wid * 256); glds4_s(pf1, lane * 64, lds0 + G1_PFOFF + wid * 256);
    st16(xb, (u32x4){XB_EMPTY, XB_EMPTY, XB_EMPTY, XB_EMPTY}); st16(xb + 512, (u32x4){XB_EMPTY, XB_EMPTY, XB_EMPTY, XB_EMPTY});
    WAIT_V(6); MMA();
    LOADF(2, 0, a1); WAIT_V(4); MMA();
    LOADF(3, 1, a1); MMA_(G == 0);
    STAMP(0, 2);
#undef STAGE_A
#undef STAGE_B
#undef LOADF
#undef MMA
#undef MMA_
    const int rt0 = mt * 16 + wq * 4;
    char* gO = (char*)act + lane * 16;
    if (!QKV) {
        LAS u32x2* ex = (LAS u32x2*)(lds + wq * 2048) + lane;
        u32x2 e[4];
#pragma unroll
        for (int i = 0; i < 4; ++i) e[i] = (u32x2){relu2(pack_bf16(accE[i][0], accE[i][1])), relu2(pack_bf16(accE[i][2], accE[i][3]))};
        if (G == 0) {
#pragma unroll
            for (int i = 0; i < 4; ++i) ex[i * 64] = e[i];
            WAIT_L(0);
        }
        BAR;
#pragma unroll
        for (int i = 0; i < 4; ++i)
#pragma unroll
            for (int j = 0; j < 3; ++j) {
                const f32x4 a = acc[i][2 * j], b = acc[i][2 * j + 1];
                st16(gO + act_block(rt0 + i, ((D_ + cb0) >> 5) + 2 * j + G),
                     (u32x4){relu2(pack_bf16(a[0], a[1])), relu2(pack_bf16(a[2], a[3])), relu2(pack_bf16(b[0], b[1])), relu2(pack_bf16(b[2], b[3]))});
            }
        if (G == 1) {
#pragma unroll
            for (int i = 0; i < 4; ++i) {
                const u32x2 o = ex[i * 64];
                st16(gO + act_block(rt0 + i, (D_ + cex) >> 5), (u32x4){o[0], o[1], e[i][0], e[i][1]});
            }
        }
    } else {
        u32x4 Qf[4], Kf[5], Vf[5];
#pragma unroll
        for (int i = 0; i < 4; ++i) {
            Qf[i] = pack8(acc[i][0], acc[i][1]); Kf[i + 1] = pack8(acc[i][2], acc[i][3]); Vf[i + 1] = pack8(acc[i][4], acc[i][5]);
        }
        LAS u32x4* ex = (LAS u32x4*)(lds + wid * 2048) + lane;
        ex[0] = Kf[4]; ex[64] = Vf[4];
        const int par = fr & 1;
        const bool cura = fr - 4 * fq - par >= 0, curb = fr - 4 * fq - par - 2 >= 0;
        const f32x4 z = {0.f, 0.f, 0.f, 0.f};
        auto half = [](unsigned a, unsigned b) { return __builtin_bit_cast(s16x4, (u32x2){a, b}); };
        auto attend = [&](const int i) {
            const f32x4 s0 = mfma16(__builtin_bit_cast(bf16x8, Kf[i]), __builtin_bit_cast(bf16x8, Qf[i]), z);
            const f32x4 s1 = mfma16(__builtin_bit_cast(bf16x8, Kf[i + 1]), __builtin_bit_cast(bf16x8, Qf[i]), z);
            float la = cura ? (par ? s1[1] : s1[0]) : (par ? s0[1] : s0[0]);
            float lb = curb ? (par ? s1[3] : s1[2]) : (par ? s0[3] : s0[2]);
            const unsigned l1 = pack_bf16(la, lb);
            const unsigned l2 = pack_bf16(bf_lo(l1) * 0.17677669529663687f, bf_hi(l1) * 0.17677669529663687f);
            la = bf_lo(l2); lb = bf_hi(l2);
            if (i == 0 && rt0 == 0) { la = cura ? la : -1e9f; lb = curb ? lb : -1e9f; }
            const float mx = xred4<true>(vmax(la, lb));
            const float ea = __expf(la - mx), eb = __expf(lb - mx);
            const float inv = __builtin_amdgcn_rcpf(xred4<false>(ea + eb));
            const unsigned pw = pack_bf16(ea * inv, eb * inv);
            const unsigned wa = (pw & 0xffffu) << (16 * par), wb = (pw >> 16) << (16 * par);
            const s16x4 P0 = half(cura ? 0u : wa, curb ? 0u : wb), P1 = half(cura ? wa : 0u, curb ? wb : 0u);
            f32x4 o0 = __builtin_amdgcn_mfma_f32_16x16x16bf16_1k(half(Vf[i][0], Vf[i][1]), P0, z, 0, 0, 0);
            f32x4 o1 = __builtin_amdgcn_mfma_f32_16x16x16bf16_1k(half(Vf[i][2], Vf[i][3]), P0, z, 0, 0, 0);
            o0 = __builtin_amdgcn_mfma_f32_16x16x16bf16_1k(half(Vf[i + 1][0], Vf[i + 1][1]), P1, o0, 0, 0, 0);
            o1 = __builtin_amdgcn_mfma_f32_16x16x16bf16_1k(half(Vf[i + 1][2], Vf[i + 1][3]), P1, o1, 0, 0, 0);
            st16(gO + act_block(rt0 + i, ct * 2 + G), pack8(o0, o1));
        };
        attend(3); attend(2); attend(1);
        WAIT_L(0); BAR;
        STAMP(0, 3);
        if (wq == 0) { Kf[0] = pack8(accE[0], accE[1]); Vf[0] = pack8(accE[2], accE[3]); }
        else { Kf[0] = ex[-128]; Vf[0] = ex[-64]; }
        attend(0);
    }
    STAMP(0, 4);
    STAMP(0, 5);
}

__global__ __launch_bounds__(512, 2) void gemm1_kernel(const __hip_bfloat16* __restrict__ xn, const __hip_bfloat16* __restrict__ wqkv,
                                                       const __hip_bfloat16* __restrict__ w1, __hip_bfloat16* __restrict__ act,
                                                       const __hip_bfloat16* __restrict__ wo, const __hip_bfloat16* __restrict__ w2,
                                                       unsigned* __restrict__ xbuf) {
    extern __shared__ __attribute__((aligned(16))) unsigned char smem[];
    LAS unsigned char* lds = (LAS unsigned char*)smem;
    const int xcd = blockIdx.x & 7, j = blockIdx.x >> 3;
    const int ct = xcd * 2 + (j & 1), mt = j >> 2;
    const int wid = __builtin_amdgcn_readfirstlane(threadIdx.x >> 6);
    const char* pf0 = blockIdx.x < 224 ? (const char*)w2 + (size_t)blockIdx.x * 32768 : (const char*)wo + (size_t)(blockIdx.x - 224) * 32768;
    const char* pf1 = (const char*)wo + (1u << 20) + (size_t)((blockIdx.x * 8 + wid) & 255) * 4096;
    u32x4* xb = (u32x4*)xbuf + blockIdx.x * 1024 + threadIdx.x;
    if ((j >> 1) & 1) gemm1_body<true>(lds, xn, wqkv, act, pf0, pf1, xb, mt, ct);
    else gemm1_body<false>(lds, xn, w1, act, pf0, pf1, xb, mt, ct);
}

#define PROBE_GAP 4
constexpr int G2_NS = 5, G2_STAGE = 32768, G2_LDS = G2_NS * G2_STAGE, G2_NT = KACT / 64 / 2;
constexpr int G2_XOFF = (G2_NT % G2_NS) * G2_STAGE, G2_ROFF = 65536;
static_assert(G2_XOFF >= 32768 && G2_XOFF + 16384 <= G2_ROFF, "LDS map: probes at 0 and 128 KiB, 32 KiB each");

__global__ __launch_bounds__(768, 3) void gemm2_kernel(const __hip_bfloat16* __restrict__ act, const __hip_bfloat16* __restrict__ wo,
                                                       const __hip_bfloat16* __restrict__ w2, const __hip_bfloat16* __restrict__ x,
                                                       __hip_bfloat16* __restrict__ out, unsigned* __restrict__ xbuf) {
    extern __shared__ __attribute__((aligned(16))) unsigned char smem[];
    LAS unsigned char* lds = (LAS unsigned char*)smem;
    const int tid = threadIdx.x, wid = __builtin_amdgcn_readfirstlane(tid >> 6), lane = tid & 63;
    const int G = wid >> 2, wq = wid & 3, wm = wq >> 1, wn = wq & 1, fr = lane & 15, fq = lane >> 4;
    const int xcd = blockIdx.x & 7, jb = blockIdx.x >> 3;
    const int sp = xcd & 1, mt = (xcd >> 1) * 4 + (jb & 3), nt = jb >> 2;
    const int m0 = mt * 128, n0 = nt * 128, kt0 = sp * G2_NT;
    const int pair = (mt * 8 + nt) * 2;

    if (G == 2) {
        const unsigned voffA = (unsigned)(wq * 1024 + lane * 16);
        const unsigned voffB = (unsigned)((8 * wq + (lane >> 3)) * (D_ * 2) + (((lane & 7) ^ ((lane >> 4) << 1)) << 4));
        const char* gA = (const char*)act + act_block(mt * 8, kt0 * 2);
        const unsigned ldsw = (unsigned)(uintptr_t)lds + wq * 1024;
        auto stage = [&](int t, int slot) {
            const int kt = kt0 + t;
            const char* gB = (kt < 16 ? (const char*)wo + (size_t)kt * (64 * D_ * 2) : (const char*)w2 + (size_t)(kt - 16) * (64 * D_ * 2)) + n0 * 2;
            const unsigned d = ldsw + slot * G2_STAGE;
#pragma unroll
            for (int u = 0; u < 4; ++u) glds_s(gB + (size_t)(u & 1) * (32 * D_ * 2) + (u >> 1) * 128, voffB, d + 16384 + (u >> 1) * 8192 + (u & 1) * 4096);
#pragma unroll
            for (int u = 0; u < 4; ++u) glds_s(gA + (size_t)t * (128 * 2048) + u * 4096, voffA, d + u * 4096);
        };
        for (int t = 0; t < 4; ++t) stage(t, t);
        WAIT_V(24); BAR;
        int ss = 4;
        for (int s = 0; s < G2_NT - 4; ++s) {
            stage(s + 4, ss);
            ss = ss == G2_NS - 1 ? 0 : ss + 1;
            WAIT_V(24); BAR;
        }
        const char* gX = (const char*)x + (size_t)(m0 + (wq >> 1) * 64 + (2 * (wq & 1) + sp) * 16) * (D_ * 2) + n0 * 2;
        const unsigned voffX = (unsigned)((lane >> 4) * (D_ * 2) + (((lane & 15) ^ (lane >> 4)) << 4));
#pragma unroll
        for (int u = 0; u < 4; ++u) glds_s(gX + u * (4 * D_ * 2), voffX ^ (u << 6), (unsigned)(uintptr_t)lds + G2_XOFF + wq * 4096 + u * 1024);
        WAIT_V(20); BAR;
        WAIT_V(12); BAR;
        WAIT_V(4); BAR;
        BAR; BAR;
        WAIT_V(0); BAR;
        return;
    }

    const int q4 = (lane & 15) >> 2, p4 = lane & 3;
    const int aoff[2] = {wm * 8192 + lane * 16 + sp * 2048, wm * 8192 + lane * 16 - sp * 2048};
    const int boff = 16384 + wn * 8192 + fq * 512 + q4 * 128 + ((((q4 >> 1) & 1) + 2 * (fq & 1)) << 5) + p4 * 8;
    STAMP(1, 0);
    f32x4 acc[4][4];
#pragma unroll
    for (int i = 0; i < 4; ++i)
#pragma unroll
        for (int c = 0; c < 4; ++c) acc[i][c] = (f32x4){0.f, 0.f, 0.f, 0.f};
    bf16x8 Af[4][2], Bf[4][2];

    BAR;
    if (G == 1) BAR;
    STAMP(1, 1);
    int slot = G;
    auto step = [&]() {
        LAS unsigned char* sb = lds + slot * G2_STAGE;
#pragma unroll
        for (int k = 0; k < 2; ++k) {
#pragma unroll
            for (int c = 0; c < 4; ++c) Bf[c][k] = lds_tr(sb + (boff ^ (c << 5)) + k * 4096, 2048);
#pragma unroll
            for (int i = 0; i < 4; ++i) Af[i][k] = lds_b128(sb + aoff[i & 1] + i * 2048 + k * 1024);
        }
        WAIT_L(0); BAR;
        __builtin_amdgcn_s_setprio(1);
#pragma unroll
        for (int k = 0; k < 2; ++k)
#pragma unroll
            for (int c = 0; c < 4; ++c)
#pragma unroll
                for (int i = 0; i < 4; ++i) acc[i][c] = mfma16(Bf[c][k], Af[i][k], acc[i][c]);
        __builtin_amdgcn_s_setprio(0);
        BAR; SCHED;
        slot += 2; if (slot >= G2_NS) slot -= G2_NS;
    };
#pragma unroll 1
    for (int t = 0; t < G2_NT / 2; ++t) step();
    STAMP(1, 2);
    LAS f32x4* r1 = (LAS f32x4*)(lds + G2_ROFF + wq * 8192) + lane;
    LAS f32x4* r2 = r1 + 2048;
    const int xo = wq * 4096 + lane * 16;
    if (G == 1) {
        const __amdgpu_buffer_rsrc_t rs = __builtin_amdgcn_make_buffer_rsrc((void*)xbuf, 0, 256 * 16384, 0x00020000);
        f32x4 t[8];
#pragma unroll
        for (int e = 0; e < 8; ++e) t[e] = r1[e * 64];
        SCHED;
#pragma unroll
        for (int e = 0; e < 4; ++e)
            __builtin_amdgcn_raw_buffer_store_b128(pack8(acc[(e & 2) + 1][2 * (e & 1)] + t[2 * e], acc[(e & 2) + 1][2 * (e & 1) + 1] + t[2 * e + 1]),
                                                   rs, (pair + sp) * 16384 + xo + e * 1024, 0, 16);
#pragma unroll
        for (int u = 0; u < 2; ++u)
#pragma unroll
            for (int c = 0; c < 4; ++c) r2[(u * 4 + c) * 64] = acc[2 * u][c];
        WAIT_L(0); BAR;
        STAMP(1, 3);
        return;
    }
#pragma unroll
    for (int u = 0; u < 2; ++u)
#pragma unroll
        for (int c = 0; c < 4; ++c) r1[(u * 4 + c) * 64] = acc[2 * u + 1][c];
    WAIT_L(0); BAR;
#define PIN8 asm volatile("" : "+v"(acc[0][0]), "+v"(acc[0][1]), "+v"(acc[0][2]), "+v"(acc[0][3]), "+v"(acc[2][0]), "+v"(acc[2][1]), "+v"(acc[2][2]), "+v"(acc[2][3]))
    BAR;
    STAMP(1, 3);
    u32x4 rr[4];
    {
        const char* gx = (const char*)xbuf + (size_t)(pair + (sp ^ 1)) * 16384;
        auto probe = [&](int k) {
            const unsigned d = (unsigned)(uintptr_t)lds + (k >> 1) * 131072 + wq * 8192 + (k & 1) * 4096;
#pragma unroll
            for (int e = 0; e < 4; ++e)
                asm volatile("s_mov_b32 m0, %2\n\ts_nop 0\n\tglobal_load_lds_dwordx4 %0, %1 sc1" ::"v"(xo), "s"(gx + e * 1024), "s"(d + e * 1024) : "memory");
        };
        probe(0);
#pragma unroll
        for (int u = 0; u < 2; ++u)
#pragma unroll
            for (int c = 0; c < 4; ++c) {
                const u32x2 xr = *(LAS u32x2*)(lds + G2_XOFF + (wm * 2 + u) * 4096 + fr * 256 + (((wn * 8 + c * 2 + (fq >> 1)) ^ fr) << 4) + (fq & 1) * 8);
                acc[2 * u][c] += r2[(u * 4 + c) * 64] + (f32x4){bf_lo(xr[0]), bf_hi(xr[0]), bf_lo(xr[1]), bf_hi(xr[1])};
            }
        PIN8;
#pragma unroll
        for (int k = 1; k < 4; ++k) { probe(k); __builtin_amdgcn_s_sleep(PROBE_GAP); }
        for (int spin = 0; spin < (1 << 16); ++spin) {
            const int k = spin & 3;
            WAIT_V(12);
            LAS u32x4* p = (LAS u32x4*)(lds + (k >> 1) * 131072 + wq * 8192 + (k & 1) * 4096) + lane;
            unsigned all = 0u;
#pragma unroll
            for (int e = 0; e < 4; ++e) {
                rr[e] = p[e * 64];
                all = max(max(max(all, rr[e][0]), max(rr[e][1], rr[e][2])), rr[e][3]);
            }
            if (__builtin_amdgcn_ballot_w64(all == XB_EMPTY) == 0) break;
            probe(k);
        }
    }
    STAMP(1, 4);
#pragma unroll
    for (int u = 0; u < 2; ++u) {
        char* gO = (char*)out + (size_t)(m0 + wm * 64 + (2 * u + sp) * 16 + fr) * (D_ * 2) + (n0 + wn * 64 + (fq & 1) * 16 + (fq >> 1) * 8) * 2;
#pragma unroll
        for (int j = 0; j < 2; ++j) {
            const u32x4 r = rr[u * 2 + j];
            const f32x4 a = acc[2 * u][2 * j], b = acc[2 * u][2 * j + 1];
            const auto rx = __builtin_amdgcn_permlane16_swap(pack_bf16(a[0] + bf_lo(r[0]), a[1] + bf_hi(r[0])), pack_bf16(b[0] + bf_lo(r[2]), b[1] + bf_hi(r[2])), false, false);
            const auto ry = __builtin_amdgcn_permlane16_swap(pack_bf16(a[2] + bf_lo(r[1]), a[3] + bf_hi(r[1])), pack_bf16(b[2] + bf_lo(r[3]), b[3] + bf_hi(r[3])), false, false);
            st16(gO + j * 64, (u32x4){rx[0], ry[0], rx[1], ry[1]});
        }
    }
    STAMP(1, 5);
}

extern "C" void kernel_launch(void* const* d_in, const int* in_sizes, int n_in, void* d_out, int out_size, void* d_ws,
                              size_t ws_size, hipStream_t stream) {
    const __hip_bfloat16* x = (const __hip_bfloat16*)d_in[0];
    const __hip_bfloat16* wqkv = (const __hip_bfloat16*)d_in[1];
    const __hip_bfloat16* wo = (const __hip_bfloat16*)d_in[2];
    const __hip_bfloat16* w1 = (const __hip_bfloat16*)d_in[4];
    const __hip_bfloat16* w2 = (const __hip_bfloat16*)d_in[5];
    char* ws = (char*)d_ws;
    __hip_bfloat16* xn = (__hip_bfloat16*)ws;
    __hip_bfloat16* act = (__hip_bfloat16*)(ws + (4u << 20));
    unsigned* xbuf = (unsigned*)(ws + (24u << 20));
    (void)hipFuncSetAttribute((const void*)gemm1_kernel, hipFuncAttributeMaxDynamicSharedMemorySize, G1_LDS);
    (void)hipFuncSetAttribute((const void*)gemm2_kernel, hipFuncAttributeMaxDynamicSharedMemorySize, G2_LDS);
    ln_kernel<<<S_ / 4, 256, 0, stream>>>(x, xn);
    gemm1_kernel<<<256, 512, G1_LDS, stream>>>(xn, wqkv, w1, act, wo, w2, xbuf);
    gemm2_kernel<<<256, 768, G2_LDS, stream>>>(act, wo, w2, x, (__hip_bfloat16*)d_out, xbuf);
}
```

```cpp
#include <hip/hip_runtime.h>
#include <hip/hip_bf16.h>
#include <stdint.h>

#define LAS __attribute__((address_space(3)))
typedef __attribute__((ext_vector_type(8))) short bf16x8;
typedef __attribute__((ext_vector_type(4))) short s16x4;
typedef __attribute__((ext_vector_type(4))) float f32x4;
typedef __attribute__((ext_vector_type(2))) float f32x2;
typedef __attribute__((ext_vector_type(2))) __bf16 bf16x2_t;
typedef __attribute__((ext_vector_type(4))) unsigned u32x4;
typedef __attribute__((ext_vector_type(2))) unsigned u32x2;

constexpr int S_ = 2048, D_ = 1024, DFF = 3584, KACT = D_ + DFF;
__device__ __forceinline__ size_t act_block(int rt, int kb) { return (size_t)((kb >> 1) * 128 + rt) * 2048 + (size_t)(kb & 1) * 1024; }

constexpr unsigned XB_EMPTY = 0xffffffffu;

#define WAIT_V(n) asm volatile("s_waitcnt vmcnt(" #n ")" ::: "memory")
#define WAIT_L(n) asm volatile("s_waitcnt lgkmcnt(" #n ")" ::: "memory")
#define BAR do { __builtin_amdgcn_s_barrier(); asm volatile("" ::: "memory"); } while (0)
#define SCHED __builtin_amdgcn_sched_barrier(0)

__device__ __forceinline__ void glds_s(const void* sbase, unsigned voff, unsigned ldsaddr) {
    asm volatile("s_mov_b32 m0, %2\n\ts_nop 0\n\tglobal_load_lds_dwordx4 %0, %1" ::"v"(voff), "s"(sbase), "s"(ldsaddr) : "memory");
}
template <int IMM>
__device__ __forceinline__ void glds_i(const void* sbase, unsigned voff, unsigned la) {
    asm volatile("s_add_i32 m0, %2, %3\n\ts_nop 0\n\tglobal_load_lds_dwordx4 %0, %1" ::"v"(voff), "s"(sbase), "s"(la), "n"(IMM) : "memory", "scc");
}
__device__ __forceinline__ void glds_a(const void* sbase, unsigned voff, unsigned la, unsigned lb) {
    asm volatile("s_add_i32 m0, %2, %3\n\ts_nop 0\n\tglobal_load_lds_dwordx4 %0, %1" ::"v"(voff), "s"(sbase), "s"(la), "s"(lb) : "memory", "scc");
}
__device__ __forceinline__ void glds4_s(const void* sbase, unsigned voff, unsigned ldsaddr) {
    asm volatile("s_mov_b32 m0, %2\n\ts_nop 0\n\tglobal_load_lds_dword %0, %1" ::"v"(voff), "s"(sbase), "s"(ldsaddr) : "memory");
}
__device__ __forceinline__ unsigned pack_bf16(float a, float b) {
    f32x2 v = {a, b};
    bf16x2_t r = __builtin_convertvector(v, bf16x2_t);
    return __builtin_bit_cast(unsigned, r);
}
__device__ __forceinline__ float bf_lo(unsigned u) { return __builtin_bit_cast(float, u << 16); }
__device__ __forceinline__ float bf_hi(unsigned u) { return __builtin_bit_cast(float, u & 0xffff0000u); }
__device__ __forceinline__ float rbf(float a) { return bf_lo(pack_bf16(a, a)); }
__device__ __forceinline__ bf16x8 lds_b128(LAS unsigned char* p) { return *(LAS bf16x8*)p; }
__device__ __forceinline__ bf16x8 lds_tr(LAS unsigned char* p, int hi_off) {
    s16x4 lo = __builtin_amdgcn_ds_read_tr16_b64_v4i16((LAS s16x4*)p);
    s16x4 hi = __builtin_amdgcn_ds_read_tr16_b64_v4i16((LAS s16x4*)(p + hi_off));
    return __builtin_shufflevector(lo, hi, 0, 1, 2, 3, 4, 5, 6, 7);
}
__device__ __forceinline__ f32x4 mfma16(bf16x8 a, bf16x8 b, f32x4 c) {
    return __builtin_amdgcn_mfma_f32_16x16x32_bf16(a, b, c, 0, 0, 0);
}
__device__ __forceinline__ u32x4 pack8(f32x4 a, f32x4 b) {
    return (u32x4){pack_bf16(a[0], a[1]), pack_bf16(a[2], a[3]), pack_bf16(b[0], b[1]), pack_bf16(b[2], b[3])};
}
__device__ __forceinline__ unsigned relu2(unsigned u) {
    typedef __attribute__((ext_vector_type(2))) short s16x2;
    return __builtin_bit_cast(unsigned, __builtin_elementwise_max(__builtin_bit_cast(s16x2, u), (s16x2){0, 0}));
}
__device__ __forceinline__ void st16(void* p, u32x4 v) { asm volatile("global_store_dwordx4 %0, %1, off sc0 sc1\n\ts_nop 1" ::"v"(p), "v"(v) : "memory"); }
__device__ __forceinline__ float u2f(unsigned u) { return __builtin_bit_cast(float, u); }
__device__ __forceinline__ unsigned f2u(float f) { return __builtin_bit_cast(unsigned, f); }
__device__ __forceinline__ float vmax(float a, float b) { float r; asm("v_max_f32 %0, %1, %2" : "=v"(r) : "v"(a), "v"(b)); return r; }
template <bool MAX>
__device__ __forceinline__ float xred4(float v) {
    const auto a = __builtin_amdgcn_permlane16_swap(f2u(v), f2u(v), false, false);
    v = MAX ? vmax(u2f(a[0]), u2f(a[1])) : u2f(a[0]) + u2f(a[1]);
    const auto b = __builtin_amdgcn_permlane32_swap(f2u(v), f2u(v), false, false);
    return MAX ? vmax(u2f(b[0]), u2f(b[1])) : u2f(b[0]) + u2f(b[1]);
}

template <int CTRL>
__device__ __forceinline__ float dpp_add(float v) { return v + u2f(__builtin_amdgcn_update_dpp(0u, f2u(v), CTRL, 0xf, 0xf, true)); }
__device__ __forceinline__ float wave_sum(float v) {
    return xred4<false>(dpp_add<0x140>(dpp_add<0x141>(dpp_add<0x4e>(dpp_add<0xb1>(v)))));
}

__global__ __launch_bounds__(256) void ln_kernel(const __hip_bfloat16* __restrict__ x, __hip_bfloat16* __restrict__ xn) {
    const int lane = threadIdx.x & 63, row = blockIdx.x * 4 + (threadIdx.x >> 6);
    const u32x4* src = (const u32x4*)(x + (size_t)row * D_);
    u32x4 v0 = src[lane], v1 = src[64 + lane];
    float f[16];
#pragma unroll
    for (int i = 0; i < 4; ++i) { f[2 * i] = bf_lo(v0[i]); f[2 * i + 1] = bf_hi(v0[i]); f[8 + 2 * i] = bf_lo(v1[i]); f[9 + 2 * i] = bf_hi(v1[i]); }
    float s = 0.f, s1 = 0.f;
#pragma unroll
    for (int i = 0; i < 8; ++i) { s += f[i]; s1 += f[8 + i]; }
    const float mean = wave_sum(s + s1) * (1.0f / D_);
    float q = 0.f, q1 = 0.f;
#pragma unroll
    for (int i = 0; i < 8; ++i) { const float d = f[i] - mean, e = f[8 + i] - mean; q += d * d; q1 += e * e; }
    q = wave_sum(q + q1);
    const float mb = rbf(mean);
    const float ri = 1.0f / rbf(sqrtf(rbf(rbf(q * (1.0f / D_)) + 1e-5f)));
    u32x4 o0, o1;
#pragma unroll
    for (int i = 0; i < 4; ++i) {
        const unsigned d0 = pack_bf16(f[2 * i] - mb, f[2 * i + 1] - mb), d1 = pack_bf16(f[8 + 2 * i] - mb, f[9 + 2 * i] - mb);
        o0[i] = pack_bf16(bf_lo(d0) * ri, bf_hi(d0) * ri);
        o1[i] = pack_bf16(bf_lo(d1) * ri, bf_hi(d1) * ri);
    }
    u32x4* dst = (u32x4*)(xn + (size_t)row * D_);
    st16(dst + lane, o0); st16(dst + 64 + lane, o1);
}

constexpr int G1_ABUF = 34816, G1_BOFF = 3 * G1_ABUF, G1_BBUF = 14336, G1_PFOFF = G1_BOFF + 4 * G1_BBUF, G1_LDS = G1_PFOFF + 2048;

template <bool QKV>
__device__ __forceinline__ void gemm1_body(LAS unsigned char* lds, const __hip_bfloat16* __restrict__ xn,
                                           const __hip_bfloat16* __restrict__ w, __hip_bfloat16* __restrict__ act,
                                           const char* __restrict__ pf0, const char* __restrict__ pf1, u32x4* __restrict__ xb,
                                           const int mt, const int ct) {
    constexpr int LDB = QKV ? 3 * D_ : DFF;
    const int tid = threadIdx.x, wid = __builtin_amdgcn_readfirstlane(tid >> 6), lane = tid & 63;
    const int G = wid >> 2, wq = wid & 3, fr = lane & 15, fq = lane >> 4;
    const int m0 = mt * 256;
    int cb0, cstep, cex;
    if (QKV) { cb0 = ct * 64; cstep = D_; cex = 0; }
    else { const int n0 = ct * 224; cb0 = n0 + ((ct & 1) ? 32 : 0); cstep = 64; cex = (ct & 1) ? n0 : n0 + 192; }

    const unsigned lds0 = (unsigned)(uintptr_t)lds;
    const unsigned voffA = (unsigned)((8 * wid + (lane >> 3)) * (D_ * 2) + (((lane & 7) ^ ((4 * wid + (lane >> 4)) & 7)) << 4));
    const unsigned voffB = (unsigned)((8 * wq + (lane >> 3)) * (LDB * 2) + (((lane & 7) ^ ((((lane >> 4) & 1) + 2 * (wq & 1)) << 1)) << 4));
    unsigned voff2 = voffB;
    if (G) {
        if (QKV) voff2 = (unsigned)((4 * wq + ((lane >> 3) & 3)) * (D_ * 2) + (((lane & 7) ^ ((2 * wq + ((lane >> 4) & 1)) & 7)) << 4));
        else voff2 = (unsigned)((8 * wq + ((lane >> 2) & 7)) * (LDB * 2) + ((((lane >> 1) & 1) ^ (wq & 1)) << 5) + ((lane & 1) << 4));
    }
    const char* gA = (const char*)xn + (size_t)m0 * (D_ * 2);
    const char* gB1 = (const char*)w + (size_t)(cb0 + G * cstep) * 2;
    const char* gB2 = G ? (QKV ? (const char*)xn + (size_t)(m0 >= 16 ? m0 - 16 : 0) * (D_ * 2) : (const char*)w + (size_t)cex * 2)
                        : (const char*)w + (size_t)(cb0 + 2 * cstep) * 2;
    constexpr size_t KSB = (size_t)32 * LDB * 2;
    const unsigned dB1 = lds0 + G1_BOFF + G * 4096 + wq * 1024;
    const unsigned dB2 = G ? (QKV ? lds0 + 32768 + wq * 512 : lds0 + G1_BOFF + 12288 + wq * 512) : lds0 + G1_BOFF + 8192 + wq * 1024;
    const bool halo = QKV && G;
    const int lim2 = G ? 32 : 64;
#define STAGE_A(t, kh, ab) do { \
        glds_s(gA + (size_t)(t) * 128 + (size_t)(2 * (kh)) * (64 * D_ * 2), voffA, lds0 + (ab) + (kh) * 16384 + wid * 1024); \
        glds_s(gA + (size_t)(t) * 128 + (size_t)(2 * (kh) + 1) * (64 * D_ * 2), voffA, lds0 + (ab) + (kh) * 16384 + 8192 + wid * 1024); } while (0)
#define STAGE_B(h, s, t, ab) do { \
        glds_s(gB1 + (size_t)(h) * KSB, voffB, dB1 + (s) * G1_BBUF); \
        if (lane < lim2) glds_s(gB2 + (halo ? (size_t)(t) * 128 : (size_t)(h) * KSB), voff2, dB2 + (halo ? (unsigned)(ab) : (unsigned)((s) * G1_BBUF))); } while (0)

    const int q4 = (lane & 15) >> 2, p4 = lane & 3;
    const int aswz = (fq ^ ((fr >> 1) & 7)) << 4;
    const int oA = (wq * 64 + fr) * 128 + aswz, oH = 32768 + fr * 128 + aswz;
    const int bsw = ((q4 >> 1) & 1) + 2 * (fq & 1);
    const int bbase = G1_BOFF + fq * 1024 + q4 * 128 + p4 * 8;
    LAS unsigned char* pB[2] = {lds + bbase + (((2 * G) ^ bsw) << 5), lds + bbase + (((2 * G + 1) ^ bsw) << 5)};
    LAS unsigned char* pEB = lds + G1_BOFF + 12288 + fq * 512 + q4 * 64 + ((G ^ (fq & 1)) << 5) + p4 * 8;

    f32x4 acc[4][6], accE[4];
#pragma unroll
    for (int i = 0; i < 4; ++i) {
        accE[i] = (f32x4){0.f, 0.f, 0.f, 0.f};
#pragma unroll
        for (int c = 0; c < 6; ++c) acc[i][c] = (f32x4){0.f, 0.f, 0.f, 0.f};
    }
    bf16x8 Af[4], Bf[6], Ex;

#define LOADF(s, kh, ab) do { \
        _Pragma("unroll") for (int _c = 0; _c < 6; ++_c) Bf[_c] = lds_tr(pB[_c & 1] + (s) * G1_BBUF + (_c >> 1) * 4096, 512); \
        if (QKV) Ex = lds_b128(lds + (ab) + (oH ^ ((kh) << 6))); else Ex = lds_tr(pEB + (s) * G1_BBUF, 256); \
        _Pragma("unroll") for (int _i = 0; _i < 4; ++_i) Af[_i] = lds_b128(lds + (ab) + (oA ^ ((kh) << 6)) + _i * 2048); \
        SCHED; } while (0)
#define MMA() MMA_(true)
#define MMA_(closing) do { WAIT_L(0); BAR; __builtin_amdgcn_s_setprio(1); \
        _Pragma("unroll") for (int _c = 0; _c < 6; ++_c) _Pragma("unroll") for (int _i = 0; _i < 4; ++_i) \
            acc[_i][_c] = (QKV && _c >= 4) ? mfma16(Af[_i], Bf[_c], acc[_i][_c]) : mfma16(Bf[_c], Af[_i], acc[_i][_c]); \
        if (QKV) { if (wq == 0) { accE[0] = mfma16(Bf[2], Ex, accE[0]); accE[1] = mfma16(Bf[3], Ex, accE[1]); \
                                  accE[2] = mfma16(Ex, Bf[4], accE[2]); accE[3] = mfma16(Ex, Bf[5], accE[3]); } } \
        else { _Pragma("unroll") for (int _i = 0; _i < 4; ++_i) accE[_i] = mfma16(Ex, Af[_i], accE[_i]); } \
        asm volatile("" : "+v"(accE[0]), "+v"(accE[1]), "+v"(accE[2]), "+v"(accE[3])); \
        __builtin_amdgcn_s_setprio(0); if (closing) BAR; SCHED; } while (0)

    STAGE_A(0, 0, 0); STAGE_A(0, 1, 0); STAGE_B(0, 0, 0, 0);
    STAGE_A(1, 0, G1_ABUF); STAGE_B(1, 1, 1, G1_ABUF);
    STAGE_A(1, 1, G1_ABUF); STAGE_B(2, 2, 1, G1_ABUF);
    WAIT_V(8); BAR;
    if (G == 1) BAR;
    int a0 = 0, a1 = G1_ABUF, a2 = 2 * G1_ABUF;
    {
        const char* pA = gA + 256;
        const char* p1 = gB1 + 3 * KSB;
        const char* p2 = gB2 + (halo ? (size_t)256 : 3 * KSB);
        const unsigned inc0 = halo ? 0u : (unsigned)KSB, inc1 = halo ? 128u : (unsigned)KSB, hm = halo ? ~0u : 0u, adw = lds0 + wid * 1024;
        unsigned vA[4], cs[4];
#pragma unroll
        for (int u = 0; u < 4; ++u) { vA[u] = voffA + u * (64 * D_ * 2); cs[u] = dB2 + (halo ? 0u : (unsigned)(u * G1_BBUF)); }
#define STAGE(kh, s, aw, ah) do { \
        glds_i<(kh) * 16384>(pA, vA[2 * (kh)], aw); glds_i<(kh) * 16384 + 8192>(pA, vA[2 * (kh) + 1], aw); glds_i<(s) * G1_BBUF>(p1, voffB, dB1); \
        if (lane < lim2) { if (QKV) glds_a(p2, voff2, ah, cs[s]); else glds_i<(s) * G1_BBUF>(p2, voff2, dB2); } \
        p1 += KSB; p2 += (kh) ? inc1 : inc0; if (kh) pA += 128; \
        asm volatile("" : "+s"(pA), "+s"(p1), "+s"(p2)); } while (0)
        for (int t = 0; t < 14; t += 2) {
            const unsigned w2 = adw + a2, w0 = adw + a0, h2 = a2 & hm, h0 = a0 & hm;
            LOADF(0, 0, a0); STAGE(0, 3, w2, h2); WAIT_V(8); MMA();
            LOADF(1, 1, a0); STAGE(1, 0, w2, h2); WAIT_V(8); MMA();
            LOADF(2, 0, a1); STAGE(0, 1, w0, h0); WAIT_V(8); MMA();
            LOADF(3, 1, a1); STAGE(1, 2, w0, h0); WAIT_V(8); MMA();
            const int a = a0; a0 = a2; a2 = a1; a1 = a;
        }
#undef STAGE
    }
    LOADF(0, 0, a0); STAGE_B(31, 3, 15, a1); WAIT_V(6); MMA();
    LOADF(1, 1, a0);
    glds4_s(pf0, tid * 64, lds0 + G1_PFOFF + wid * 256); glds4_s(pf1, lane * 64, lds0 + G1_PFOFF + wid * 256);
    st16(xb, (u32x4){XB_EMPTY, XB_EMPTY, XB_EMPTY, XB_EMPTY}); st16(xb + 512, (u32x4){XB_EMPTY, XB_EMPTY, XB_EMPTY, XB_EMPTY});
    WAIT_V(6); MMA();
    LOADF(2, 0, a1); WAIT_V(4); MMA();
    LOADF(3, 1, a1); MMA_(G == 0);
#undef STAGE_A
#undef STAGE_B
#undef LOADF
#undef MMA
#undef MMA_
    const int rt0 = mt * 16 + wq * 4;
    char* gO = (char*)act + lane * 16;
    if (!QKV) {
        LAS u32x2* ex = (LAS u32x2*)(lds + wq * 2048) + lane;
        u32x2 e[4];
#pragma unroll
        for (int i = 0; i < 4; ++i) e[i] = (u32x2){relu2(pack_bf16(accE[i][0], accE[i][1])), relu2(pack_bf16(accE[i][2], accE[i][3]))};
        if (G == 0) {
#pragma unroll
            for (int i = 0; i < 4; ++i) ex[i * 64] = e[i];
            WAIT_L(0);
        }
        BAR;
#pragma unroll
        for (int i = 0; i < 4; ++i)
#pragma unroll
            for (int j = 0; j < 3; ++j) {
                const f32x4 a = acc[i][2 * j], b = acc[i][2 * j + 1];
                st16(gO + act_block(rt0 + i, ((D_ + cb0) >> 5) + 2 * j + G),
                     (u32x4){relu2(pack_bf16(a[0], a[1])), relu2(pack_bf16(a[2], a[3])), relu2(pack_bf16(b[0], b[1])), relu2(pack_bf16(b[2], b[3]))});
            }
        if (G == 1) {
#pragma unroll
            for (int i = 0; i < 4; ++i) {
                const u32x2 o = ex[i * 64];
                st16(gO + act_block(rt0 + i, (D_ + cex) >> 5), (u32x4){o[0], o[1], e[i][0], e[i][1]});
            }
        }
    } else {
        u32x4 Qf[4], Kf[5], Vf[5];
#pragma unroll
        for (int i = 0; i < 4; ++i) {
            Qf[i] = pack8(acc[i][0], acc[i][1]); Kf[i + 1] = pack8(acc[i][2], acc[i][3]); Vf[i + 1] = pack8(acc[i][4], acc[i][5]);
        }
        LAS u32x4* ex = (LAS u32x4*)(lds + wid * 2048) + lane;
        ex[0] = Kf[4]; ex[64] = Vf[4];
        const int par = fr & 1;
        const bool cura = fr - 4 * fq - par >= 0, curb = fr - 4 * fq - par - 2 >= 0;
        const f32x4 z = {0.f, 0.f, 0.f, 0.f};
        auto half = [](unsigned a, unsigned b) { return __builtin_bit_cast(s16x4, (u32x2){a, b}); };
        auto attend = [&](const int i) {
            const f32x4 s0 = mfma16(__builtin_bit_cast(bf16x8, Kf[i]), __builtin_bit_cast(bf16x8, Qf[i]), z);
            const f32x4 s1 = mfma16(__builtin_bit_cast(bf16x8, Kf[i + 1]), __builtin_bit_cast(bf16x8, Qf[i]), z);
            float la = cura ? (par ? s1[1] : s1[0]) : (par ? s0[1] : s0[0]);
            float lb = curb ? (par ? s1[3] : s1[2]) : (par ? s0[3] : s0[2]);
            const unsigned l1 = pack_bf16(la, lb);
            const unsigned l2 = pack_bf16(bf_lo(l1) * 0.17677669529663687f, bf_hi(l1) * 0.17677669529663687f);
            la = bf_lo(l2); lb = bf_hi(l2);
            if (i == 0 && rt0 == 0) { la = cura ? la : -1e9f; lb = curb ? lb : -1e9f; }
            const float mx = xred4<true>(vmax(la, lb));
            const float ea = __expf(la - mx), eb = __expf(lb - mx);
            const float inv = __builtin_amdgcn_rcpf(xred4<false>(ea + eb));
            const unsigned pw = pack_bf16(ea * inv, eb * inv);
            const unsigned wa = (pw & 0xffffu) << (16 * par), wb = (pw >> 16) << (16 * par);
            const s16x4 P0 = half(cura ? 0u : wa, curb ? 0u : wb), P1 = half(cura ? wa : 0u, curb ? wb : 0u);
            f32x4 o0 = __builtin_amdgcn_mfma_f32_16x16x16bf16_1k(half(Vf[i][0], Vf[i][1]), P0, z, 0, 0, 0);
            f32x4 o1 = __builtin_amdgcn_mfma_f32_16x16x16bf16_1k(half(Vf[i][2], Vf[i][3]), P0, z, 0, 0, 0);
            o0 = __builtin_amdgcn_mfma_f32_16x16x16bf16_1k(half(Vf[i + 1][0], Vf[i + 1][1]), P1, o0, 0, 0, 0);
            o1 = __builtin_amdgcn_mfma_f32_16x16x16bf16_1k(half(Vf[i + 1][2], Vf[i + 1][3]), P1, o1, 0, 0, 0);
            st16(gO + act_block(rt0 + i, ct * 2 + G), pack8(o0, o1));
        };
        attend(3); attend(2); attend(1);
        WAIT_L(0); BAR;
        if (wq == 0) { Kf[0] = pack8(accE[0], accE[1]); Vf[0] = pack8(accE[2], accE[3]); }
        else { Kf[0] = ex[-128]; Vf[0] = ex[-64]; }
        attend(0);
    }
    WAIT_V(0);
}

__global__ __launch_bounds__(512, 2) void gemm1_kernel(const __hip_bfloat16* __restrict__ xn, const __hip_bfloat16* __restrict__ wqkv,
                                                       const __hip_bfloat16* __restrict__ w1, __hip_bfloat16* __restrict__ act,
                                                       const __hip_bfloat16* __restrict__ wo, const __hip_bfloat16* __restrict__ w2,
                                                       unsigned* __restrict__ xbuf) {
    extern __shared__ __attribute__((aligned(16))) unsigned char smem[];
    LAS unsigned char* lds = (LAS unsigned char*)smem;
    const int xcd = blockIdx.x & 7, j = blockIdx.x >> 3;
    const int ct = xcd * 2 + (j & 1), mt = j >> 2;
    const int wid = __builtin_amdgcn_readfirstlane(threadIdx.x >> 6);
    const char* pf0 = blockIdx.x < 224 ? (const char*)w2 + (size_t)blockIdx.x * 32768 : (const char*)wo + (size_t)(blockIdx.x - 224) * 32768;
    const char* pf1 = (const char*)wo + (1u << 20) + (size_t)((blockIdx.x * 8 + wid) & 255) * 4096;
    u32x4* xb = (u32x4*)xbuf + blockIdx.x * 1024 + threadIdx.x;
    if ((j >> 1) & 1) gemm1_body<true>(lds, xn, wqkv, act, pf0, pf1, xb, mt, ct);
    else gemm1_body<false>(lds, xn, w1, act, pf0, pf1, xb, mt, ct);
}

constexpr int G2_NS = 5, G2_STAGE = 32768, G2_LDS = G2_NS * G2_STAGE, G2_NT = KACT / 64 / 2;
constexpr int G2_XOFF = (G2_NT % G2_NS) * G2_STAGE, G2_ROFF = 65536;

__global__ __launch_bounds__(768, 3) void gemm2_kernel(const __hip_bfloat16* __restrict__ act, const __hip_bfloat16* __restrict__ wo,
                                                       const __hip_bfloat16* __restrict__ w2, const __hip_bfloat16* __restrict__ x,
                                                       __hip_bfloat16* __restrict__ out, unsigned* __restrict__ xbuf) {
    extern __shared__ __attribute__((aligned(16))) unsigned char smem[];
    LAS unsigned char* lds = (LAS unsigned char*)smem;
    const int tid = threadIdx.x, wid = __builtin_amdgcn_readfirstlane(tid >> 6), lane = tid & 63;
    const int G = wid >> 2, wq = wid & 3, wm = wq >> 1, wn = wq & 1, fr = lane & 15, fq = lane >> 4;
    const int xcd = blockIdx.x & 7, jb = blockIdx.x >> 3;
    const int sp = xcd & 1, mt = (xcd >> 1) * 4 + (jb & 3), nt = jb >> 2;
    const int m0 = mt * 128, n0 = nt * 128, kt0 = sp * G2_NT;
    const int pair = (mt * 8 + nt) * 2;

    if (G == 2) {
        const unsigned voffA = (unsigned)(wq * 1024 + lane * 16);
        const unsigned voffB = (unsigned)((8 * wq + (lane >> 3)) * (D_ * 2) + (((lane & 7) ^ ((lane >> 4) << 1)) << 4));
        const char* gA = (const char*)act + act_block(mt * 8, kt0 * 2);
        const unsigned ldsw = (unsigned)(uintptr_t)lds + wq * 1024;
        auto stage = [&](int t, int slot) {
            const int kt = kt0 + t;
            const char* gB = (kt < 16 ? (const char*)wo + (size_t)kt * (64 * D_ * 2) : (const char*)w2 + (size_t)(kt - 16) * (64 * D_ * 2)) + n0 * 2;
            const unsigned d = ldsw + slot * G2_STAGE;
#pragma unroll
            for (int u = 0; u < 4; ++u) glds_s(gB + (size_t)(u & 1) * (32 * D_ * 2) + (u >> 1) * 128, voffB, d + 16384 + (u >> 1) * 8192 + (u & 1) * 4096);
#pragma unroll
            for (int u = 0; u < 4; ++u) glds_s(gA + (size_t)t * (128 * 2048) + u * 4096, voffA, d + u * 4096);
        };
        for (int t = 0; t < 4; ++t) stage(t, t);
        WAIT_V(24); BAR;
        int ss = 4;
        for (int s = 0; s < G2_NT - 4; ++s) {
            stage(s + 4, ss);
            ss = ss == G2_NS - 1 ? 0 : ss + 1;
            WAIT_V(24); BAR;
        }
        const char* gX = (const char*)x + (size_t)(m0 + (wq >> 1) * 64 + (2 * (wq & 1) + sp) * 16) * (D_ * 2) + n0 * 2;
        const unsigned voffX = (unsigned)((lane >> 4) * (D_ * 2) + (((lane & 15) ^ (lane >> 4)) << 4));
#pragma unroll
        for (int u = 0; u < 4; ++u) glds_s(gX + u * (4 * D_ * 2), voffX ^ (u << 6), (unsigned)(uintptr_t)lds + G2_XOFF + wq * 4096 + u * 1024);
        WAIT_V(20); BAR;
        WAIT_V(12); BAR;
        WAIT_V(4); BAR;
        BAR; BAR;
        WAIT_V(0); BAR;
        return;
    }

    const int q4 = (lane & 15) >> 2, p4 = lane & 3;
    const int aoff[2] = {wm * 8192 + lane * 16 + sp * 2048, wm * 8192 + lane * 16 - sp * 2048};
    const int boff = 16384 + wn * 8192 + fq * 512 + q4 * 128 + ((((q4 >> 1) & 1) + 2 * (fq & 1)) << 5) + p4 * 8;
    f32x4 acc[4][4];
#pragma unroll
    for (int i = 0; i < 4; ++i)
#pragma unroll
        for (int c = 0; c < 4; ++c) acc[i][c] = (f32x4){0.f, 0.f, 0.f, 0.f};
    bf16x8 Af[4][2], Bf[4][2];

    BAR;
    if (G == 1) BAR;
    int slot = G;
    auto step = [&]() {
        LAS unsigned char* sb = lds + slot * G2_STAGE;
#pragma unroll
        for (int k = 0; k < 2; ++k) {
#pragma unroll
            for (int c = 0; c < 4; ++c) Bf[c][k] = lds_tr(sb + (boff ^ (c << 5)) + k * 4096, 2048);
#pragma unroll
            for (int i = 0; i < 4; ++i) Af[i][k] = lds_b128(sb + aoff[i & 1] + i * 2048 + k * 1024);
        }
        WAIT_L(0); BAR;
        __builtin_amdgcn_s_setprio(1);
#pragma unroll
        for (int k = 0; k < 2; ++k)
#pragma unroll
            for (int c = 0; c < 4; ++c)
#pragma unroll
                for (int i = 0; i < 4; ++i) acc[i][c] = mfma16(Bf[c][k], Af[i][k], acc[i][c]);
        __builtin_amdgcn_s_setprio(0);
        BAR; SCHED;
        slot += 2; if (slot >= G2_NS) slot -= G2_NS;
    };
#pragma unroll 1
    for (int t = 0; t < G2_NT / 2; ++t) step();
    LAS f32x4* r1 = (LAS f32x4*)(lds + G2_ROFF + wq * 8192) + lane;
    LAS f32x4* r2 = r1 + 2048;
    const int xo = wq * 4096 + lane * 16;
    if (G == 1) {
        const __amdgpu_buffer_rsrc_t rs = __builtin_amdgcn_make_buffer_rsrc((void*)xbuf, 0, 256 * 16384, 0x00020000);
        f32x4 t[8];
#pragma unroll
        for (int e = 0; e < 8; ++e) t[e] = r1[e * 64];
        SCHED;
#pragma unroll
        for (int e = 0; e < 4; ++e)
            __builtin_amdgcn_raw_buffer_store_b128(
                __builtin_elementwise_min(pack8(acc[(e & 2) + 1][2 * (e & 1)] + t[2 * e], acc[(e & 2) + 1][2 * (e & 1) + 1] + t[2 * e + 1]),
                                          (u32x4){XB_EMPTY - 1, XB_EMPTY - 1, XB_EMPTY - 1, XB_EMPTY - 1}),
                rs, (pair + sp) * 16384 + xo + e * 1024, 0, 16);
#pragma unroll
        for (int u = 0; u < 2; ++u)
#pragma unroll
            for (int c = 0; c < 4; ++c) r2[(u * 4 + c) * 64] = acc[2 * u][c];
        WAIT_L(0); BAR;
        return;
    }
#pragma unroll
    for (int u = 0; u < 2; ++u)
#pragma unroll
        for (int c = 0; c < 4; ++c) r1[(u * 4 + c) * 64] = acc[2 * u + 1][c];
    WAIT_L(0); BAR;
#define PIN8 asm volatile("" : "+v"(acc[0][0]), "+v"(acc[0][1]), "+v"(acc[0][2]), "+v"(acc[0][3]), "+v"(acc[2][0]), "+v"(acc[2][1]), "+v"(acc[2][2]), "+v"(acc[2][3]))
    BAR;
    u32x4 rr[4];
    {
        const char* gx = (const char*)xbuf + (size_t)(pair + (sp ^ 1)) * 16384;
        auto probe = [&](int k) {
            const unsigned d = (unsigned)(uintptr_t)lds + (k >> 1) * 131072 + wq * 8192 + (k & 1) * 4096;
#pragma unroll
            for (int e = 0; e < 4; ++e)
                asm volatile("s_mov_b32 m0, %2\n\ts_nop 0\n\tglobal_load_lds_dwordx4 %0, %1 sc1" ::"v"(xo), "s"(gx + e * 1024), "s"(d + e * 1024) : "memory");
        };
        probe(0);
#pragma unroll
        for (int u = 0; u < 2; ++u)
#pragma unroll
            for (int c = 0; c < 4; ++c) {
                const u32x2 xr = *(LAS u32x2*)(lds + G2_XOFF + (wm * 2 + u) * 4096 + fr * 256 + (((wn * 8 + c * 2 + (fq >> 1)) ^ fr) << 4) + (fq & 1) * 8);
                acc[2 * u][c] += r2[(u * 4 + c) * 64] + (f32x4){bf_lo(xr[0]), bf_hi(xr[0]), bf_lo(xr[1]), bf_hi(xr[1])};
            }
        PIN8;
#pragma unroll
        for (int k = 1; k < 4; ++k) { probe(k); __builtin_amdgcn_s_sleep(4); }
        for (int spin = 0; spin < (1 << 22); ++spin) {
            const int k = spin & 3;
            WAIT_V(12);
            LAS u32x4* p = (LAS u32x4*)(lds + (k >> 1) * 131072 + wq * 8192 + (k & 1) * 4096) + lane;
            unsigned all = 0u;
#pragma unroll
            for (int e = 0; e < 4; ++e) {
                rr[e] = p[e * 64];
                all = max(max(max(all, rr[e][0]), max(rr[e][1], rr[e][2])), rr[e][3]);
            }
            if (__builtin_amdgcn_ballot_w64(all == XB_EMPTY) == 0) break;
            probe(k);
        }
    }
#pragma unroll
    for (int u = 0; u < 2; ++u) {
        char* gO = (char*)out + (size_t)(m0 + wm * 64 + (2 * u + sp) * 16 + fr) * (D_ * 2) + (n0 + wn * 64 + (fq & 1) * 16 + (fq >> 1) * 8) * 2;
#pragma unroll
        for (int j = 0; j < 2; ++j) {
            const u32x4 r = rr[u * 2 + j];
            const f32x4 a = acc[2 * u][2 * j], b = acc[2 * u][2 * j + 1];
            const auto rx = __builtin_amdgcn_permlane16_swap(pack_bf16(a[0] + bf_lo(r[0]), a[1] + bf_hi(r[0])), pack_bf16(b[0] + bf_lo(r[2]), b[1] + bf_hi(r[2])), false, false);
            const auto ry = __builtin_amdgcn_permlane16_swap(pack_bf16(a[2] + bf_lo(r[1]), a[3] + bf_hi(r[1])), pack_bf16(b[2] + bf_lo(r[3]), b[3] + bf_hi(r[3])), false, false);
            st16(gO + j * 64, (u32x4){rx[0], ry[0], rx[1], ry[1]});
        }
    }
    WAIT_V(0);
}

extern "C" void kernel_launch(void* const* d_in, const int* in_sizes, int n_in, void* d_out, int out_size, void* d_ws,
                              size_t ws_size, hipStream_t stream) {
    const __hip_bfloat16* x = (const __hip_bfloat16*)d_in[0];
    const __hip_bfloat16* wqkv = (const __hip_bfloat16*)d_in[1];
    const __hip_bfloat16* wo = (const __hip_bfloat16*)d_in[2];
    const __hip_bfloat16* w1 = (const __hip_bfloat16*)d_in[4];
    const __hip_bfloat16* w2 = (const __hip_bfloat16*)d_in[5];
    char* ws = (char*)d_ws;
    __hip_bfloat16* xn = (__hip_bfloat16*)ws;
    __hip_bfloat16* act = (__hip_bfloat16*)(ws + (4u << 20));
    unsigned* xbuf = (unsigned*)(ws + (24u << 20));
    (void)hipFuncSetAttribute((const void*)gemm1_kernel, hipFuncAttributeMaxDynamicSharedMemorySize, G1_LDS);
    (void)hipFuncSetAttribute((const void*)gemm2_kernel, hipFuncAttributeMaxDynamicSharedMemorySize, G2_LDS);
    ln_kernel<<<S_ / 4, 256, 0, stream>>>(x, xn);
    gemm1_kernel<<<256, 512, G1_LDS, stream>>>(xn, wqkv, w1, act, wo, w2, xbuf);
    gemm2_kernel<<<256, 768, G2_LDS, stream>>>(act, wo, w2, x, (__hip_bfloat16*)d_out, xbuf);
}
```
